```python
import torch

def _softmax(x, axis=-1):
    return torch.softmax(x, dim=axis)
def _gelu(x):
    return torch.nn.functional.gelu(x, approximate="tanh")
def _silu(x):
    return torch.nn.functional.silu(x)
def _sigmoid(x):
    return torch.sigmoid(x)
def _layernorm(x, axis=-1, eps=1e-5):
    m = x.mean(dim=axis, keepdim=True)
    v = x.var(dim=axis, keepdim=True, unbiased=False)
    return (x - m) / torch.sqrt(v + eps)
def _scatter_add(tgt, idx, src, axis=0):
    return tgt.clone().index_add_(axis, idx, src)
def _segment_sum(src, seg, n):
    out = torch.zeros((n,) + src.shape[1:], dtype=src.dtype)
    return out.index_add_(0, seg, src)
def _pad(x, pw):
    flat = tuple(v for pair in reversed(pw) for v in pair)
    return torch.nn.functional.pad(x, flat)

kernel_name = 'interleave_layers__mha_block__mixer_channel_mlp__prompt'

def setup_inputs():

    _w = (lambda a: (lambda t: t.to(torch.bfloat16) if t.is_floating_point() else t)(torch.as_tensor(a)))
    def _A_setup():
        B, S, D, H = 1, 2048, 1024, 32
        return {
            "x": _w((lambda *s: torch.randn(*s, dtype=torch.bfloat16) * 0.05)(B, S, D)),
            "wqkv": _w((lambda *s: torch.randn(*s, dtype=torch.bfloat16) * 0.05)(D, 3 * D)),
            "wo": _w((lambda *s: torch.randn(*s, dtype=torch.bfloat16) * 0.05)(D, D)),
        }
    _A_in = _A_setup()

    def _B_setup():
        B, S, D, F = 1, 2048, 1024, 3584
        return {
            "x": _w((lambda *s: torch.randn(*s, dtype=torch.bfloat16) * 0.05)(B, S, D)),
            "w1": _w((lambda *s: torch.randn(*s, dtype=torch.bfloat16) * 3.2)(D, F) * (D ** -0.5)),
            "b1": _w((lambda *s: torch.randn(*s, dtype=torch.bfloat16) * 0.8)(F)),
            "w2": _w((lambda *s: torch.randn(*s, dtype=torch.bfloat16) * 0.4)(F, D) * (F ** -0.5)),
            "b2": _w((lambda *s: torch.randn(*s, dtype=torch.bfloat16) * 0.4)(D)),
        }
    _B_in = _B_setup()

    return {
        "A_x": _A_in["x"],
        "A_wqkv": _A_in["wqkv"],
        "A_wo": _A_in["wo"],
        "B_x": _B_in["x"],
        "B_w1": _B_in["w1"],
        "B_b1": _B_in["b1"],
        "B_w2": _B_in["w2"],
        "B_b2": _B_in["b2"],
    }

def reference(A_x, A_wqkv, A_wo, B_x, B_w1, B_b1, B_w2, B_b2):
    def _A_ref(x, wqkv, wo):
        B, S, D = x.shape
        H = 32
        Dh = D // H
        qkv = (lambda x, *d: x.permute(*d))((x @ wqkv).reshape(B, S, 3, H, Dh), 2, 0, 3, 1, 4)
        q, k, v = qkv[0], qkv[1], qkv[2]
        scores = torch.einsum("bhqd,bhkd->bhqk", q, k) * (Dh ** -0.5)
        p = _softmax(scores, axis=-1)
        o = torch.einsum("bhqk,bhkd->bhqd", p, v)
        return (lambda x, *d: x.permute(*d))(o, 0, 2, 1, 3).reshape(B, S, D) @ wo

    def _B_ref(x, w1, b1, w2, b2):
        y = _layernorm(x, axis=-1)
        h = _gelu(torch.einsum("bsd,df->bsf", y, w1) + b1)
        return torch.einsum("bsf,fd->bsd", h, w2) + b2

    x = A_x
    x = x + _A_ref(_layernorm(x, axis=-1), A_wqkv, A_wo)
    x = x + _B_ref(_layernorm(x, axis=-1), B_w1, B_b1, B_w2, B_b2)
    x = x + _A_ref(_layernorm(x, axis=-1), A_wqkv, A_wo)
    x = x + _B_ref(_layernorm(x, axis=-1), B_w1, B_b1, B_w2, B_b2)
    return x
```

```cpp
#include <hip/hip_runtime.h>
#include <hip/hip_bf16.h>

namespace {

typedef short bf16x8 __attribute__((ext_vector_type(8)));
typedef short s16x4 __attribute__((ext_vector_type(4)));
typedef float f32x16 __attribute__((ext_vector_type(16)));
typedef float f32x4 __attribute__((ext_vector_type(4)));
typedef float f32x2 __attribute__((ext_vector_type(2)));
typedef unsigned u32x4 __attribute__((ext_vector_type(4)));
typedef unsigned u32x2 __attribute__((ext_vector_type(2)));
typedef __bf16 bf16x2_t __attribute__((ext_vector_type(2)));
typedef unsigned short u16;
#define LAS __attribute__((address_space(3)))
typedef LAS const char* lds_cptr;
typedef LAS char* lds_ptr;
#define DI __device__ __forceinline__
#define MFMA32(a, b, c) __builtin_amdgcn_mfma_f32_32x32x16_bf16((a), (b), (c), 0, 0, 0)
#define MFMA16(a, b, c) __builtin_amdgcn_mfma_f32_16x16x32_bf16((a), (b), (c), 0, 0, 0)
#define SBAR() __builtin_amdgcn_sched_barrier(0)

constexpr int S_ = 2048, D_ = 1024, H_ = 32, F_ = 3584;
constexpr float EPS = 1e-5f;
constexpr float QSCALE = 0.17677669529663687f * 1.4426950408889634f;

constexpr size_t MB = 1u << 20;
constexpr size_t WS_X0 = 0, WS_X1 = 4 * MB, WS_X2 = 8 * MB, WS_X3 = 12 * MB;
constexpr size_t WS_Q = 16 * MB, WS_KV = 20 * MB, WS_AO = 28 * MB, WS_H = 32 * MB;
constexpr size_t WS_ST0 = 48 * MB, WS_ST1 = WS_ST0 + 256 * 1024, WS_ST2 = WS_ST1 + 256 * 1024, WS_ST3 = WS_ST2 + 256 * 1024;
constexpr size_t WS_CSQ = 49 * MB, WS_CS1 = WS_CSQ + 2 * 3072 * 4;

DI unsigned cvtpk(float lo, float hi) { f32x2 v = {lo, hi}; return __builtin_bit_cast(unsigned, __builtin_convertvector(v, bf16x2_t)); }
DI float bflo(unsigned u) { return __uint_as_float(u << 16); }
DI float bfhi(unsigned u) { return __uint_as_float(u & 0xffff0000u); }
DI float swap_add(float v) {
  auto r = __builtin_amdgcn_permlane32_swap(__float_as_uint(v), __float_as_uint(v), false, false);
  return __uint_as_float(r[0]) + __uint_as_float(r[1]);
}
DI float swap_max(float v) {
  auto r = __builtin_amdgcn_permlane32_swap(__float_as_uint(v), __float_as_uint(v), false, false);
  return __builtin_fmaxf(__uint_as_float(r[0]), __uint_as_float(r[1]));
}
DI u32x4 pack8(const f32x16& x, int s) {
  return u32x4{cvtpk(x[8 * s], x[8 * s + 1]), cvtpk(x[8 * s + 2], x[8 * s + 3]), cvtpk(x[8 * s + 4], x[8 * s + 5]), cvtpk(x[8 * s + 6], x[8 * s + 7])};
}
DI u32x4 nat2frag(u32x4 v) {
  auto r0 = __builtin_amdgcn_permlane32_swap(v[0], v[2], false, false);
  auto r1 = __builtin_amdgcn_permlane32_swap(v[1], v[3], false, false);
  return u32x4{r0[0], r1[0], r0[1], r1[1]};
}
DI bf16x8 lds_frag(lds_cptr p) { return *(const LAS bf16x8*)p; }
DI s16x4 lds_tr(lds_cptr p) { return __builtin_bit_cast(s16x4, __builtin_amdgcn_ds_read_tr16_b64_v4i16((LAS s16x4*)p)); }
DI bf16x8 cat8(s16x4 a, s16x4 b) { return __builtin_shufflevector(a, b, 0, 1, 2, 3, 4, 5, 6, 7); }
template <int N> DI void wait_vm() { asm volatile("s_waitcnt vmcnt(%0)" ::"i"(N) : "memory"); }
DI void bar() { asm volatile("s_barrier" ::: "memory"); }
DI void bar_lgkm() { asm volatile("s_waitcnt lgkmcnt(0)\n\ts_barrier" ::: "memory"); }
template <int IMM> DI void dma16(__amdgpu_buffer_rsrc_t rs, unsigned voff, unsigned soff, unsigned ldsaddr) {
  asm volatile("s_mov_b32 m0, %0\n\ts_nop 0\n\tbuffer_load_dwordx4 %1, %2, %3 offen offset:%4 lds" ::"s"(ldsaddr), "v"(voff), "s"(rs), "s"(soff), "i"(IMM) : "memory");
}
DI __amdgpu_buffer_rsrc_t mkrsrc(const void* p, unsigned bytes) { return __builtin_amdgcn_make_buffer_rsrc((void*)p, 0, bytes, 0x00020000); }

DI void load_stats(const f32x2* st, int token, int h, float& mean, float& var) {
  float s1 = 0.f, s2 = 0.f;
#pragma unroll
  for (int p = 0; p < 8; ++p) { const f32x2 v = st[(8 * h + p) * S_ + token]; s1 += v.x; s2 += v.y; }
  s1 = swap_add(s1); s2 = swap_add(s2);
  mean = s1 * (1.0f / D_);
  var = __builtin_fmaxf(s2 * (1.0f / D_) - mean * mean, 0.f);
}

__global__ __launch_bounds__(512) void prep_kernel(const u16* __restrict__ x, const u16* __restrict__ wqkv, const u16* __restrict__ w1,
                                                   u16* __restrict__ xf, f32x2* __restrict__ st, float* __restrict__ csq, float* __restrict__ cs1) {
  __shared__ float red[8][64];
  const int lane = threadIdx.x & 63, w = threadIdx.x >> 6, b = blockIdx.x;
  if (b < 128) {
    const int tt = b >> 1, p = (b & 1) * 8 + w, t = lane & 31, h = lane >> 5;
    const u16* src = x + (size_t)(tt * 32 + t) * D_ + p * 64 + h * 8;
    u32x4 v[4];
#pragma unroll
    for (int s = 0; s < 4; ++s) v[s] = *(const u32x4*)(src + s * 16);
    float s1 = 0.f, s2 = 0.f;
#pragma unroll
    for (int s = 0; s < 4; ++s) {
#pragma unroll
      for (int i = 0; i < 4; ++i) { const float a = bflo(v[s][i]), c = bfhi(v[s][i]); s1 += a + c; s2 += a * a + c * c; }
      *(u32x4*)(xf + ((size_t)(tt * 64 + p * 4 + s) * 64 + lane) * 8) = nat2frag(v[s]);
    }
    s1 = swap_add(s1); s2 = swap_add(s2);
    if (h == 0) st[p * S_ + tt * 32 + t] = f32x2{s1, s2};
  } else {
    const bool isq = b < 224;
    const int bb = isq ? b - 128 : b - 224, strip = bb >> 1, half = bb & 1, ld = isq ? 3072 : F_;
    const u16* src = (isq ? wqkv : w1) + (size_t)(half * 512 + (threadIdx.x >> 3) * 8) * ld + strip * 64 + (lane & 7) * 8;
    u32x4 v[8];
#pragma unroll
    for (int r = 0; r < 8; ++r) v[r] = *(const u32x4*)(src + (size_t)r * ld);
    float a[8];
#pragma unroll
    for (int i = 0; i < 8; ++i) a[i] = 0.f;
#pragma unroll
    for (int r = 0; r < 8; ++r)
#pragma unroll
      for (int i = 0; i < 4; ++i) { a[2 * i] += bflo(v[r][i]); a[2 * i + 1] += bfhi(v[r][i]); }
#pragma unroll
    for (int i = 0; i < 8; ++i) { a[i] += __shfl_xor(a[i], 8); a[i] += __shfl_xor(a[i], 16); a[i] += __shfl_xor(a[i], 32); }
    if (lane < 8)
#pragma unroll
      for (int i = 0; i < 8; ++i) red[w][lane * 8 + i] = a[i];
    __syncthreads();
    if (threadIdx.x < 64) {
      float s = 0.f;
#pragma unroll
      for (int i = 0; i < 8; ++i) s += red[i][threadIdx.x];
      (isq ? csq : cs1)[half * ld + strip * 64 + threadIdx.x] = s;
    }
  }
}

template <int MODE  > struct Big {
  static constexpr int LDW = MODE == 0 ? 3072 : F_;
  static constexpr int XB = 16384, WB = 24576, EB = MODE == 1 ? 4096 : 0, SLOT = XB + WB + EB, NS = 3, NT = 16;
  static constexpr int NLD = 4 + 6 + (MODE == 1 ? 1 : 0);
  static constexpr int TAB = NS * SLOT;
  static constexpr int ROWT = TAB + 2048;
  static constexpr int LDS = ROWT + 4 * 512;
};

template <int MODE>
__global__ __launch_bounds__(512, 2) void gemm_big(const u16* __restrict__ xf, const u16* __restrict__ wmat, const f32x2* __restrict__ st,
                                                   const float* __restrict__ cs, const u16* __restrict__ bias, u16* __restrict__ out0, u16* __restrict__ out1) {
  using C = Big<MODE>;
  __shared__ __attribute__((aligned(16))) char lds_[C::LDS];
  const lds_ptr lds = (lds_ptr)lds_;
  const unsigned lds0 = (unsigned)(size_t)lds;
  const int lane = threadIdx.x & 63, wid = __builtin_amdgcn_readfirstlane(threadIdx.x >> 6);
  const int xcd = blockIdx.x & 7, idx = blockIdx.x >> 3;
  const int mt = (xcd & 1) * 8 + (idx & 7), nt = (xcd >> 1) * 4 + (idx >> 3);
  int seg0, segstride, ecol;
  if (MODE == 0) { seg0 = nt * 64; segstride = 1024; ecol = 0; }
  else { seg0 = (nt >> 1) * 448 + (nt & 1) * 256; segstride = 64; ecol = (nt >> 1) * 448 + 192 + (nt & 1) * 32; }

  if (wid >= 4) {
    const int w = wid - 4;
    const __amdgpu_buffer_rsrc_t rx = mkrsrc(xf, S_ * D_ * 2), rw = mkrsrc(wmat, 1024u * C::LDW * 2);
    const unsigned vx = lane * 16;
    const unsigned vw = (((lane >> 5) * 4 + ((lane >> 2) & 3)) * C::LDW + ((lane >> 4) & 1) * 32 + (lane & 3) * 8) * 2;
    const unsigned ve = (((lane >> 4) * 4 + ((lane >> 2) & 3)) * C::LDW + (lane & 3) * 8) * 2;
    const unsigned sx0 = (unsigned)(mt * 4 + w) * 64 * 1024;
    const unsigned sw0 = (unsigned)(w * 16 * C::LDW + seg0) * 2, se0 = (unsigned)(w * 16 * C::LDW + ecol) * 2;
    auto issue = [&](int t, int slot) {
      const unsigned lb = lds0 + slot * C::SLOT;
      const unsigned sx = sx0 + t * 4096;
      dma16<0>(rx, vx, sx, lb + w * 4096); dma16<1024>(rx, vx, sx, lb + w * 4096);
      dma16<2048>(rx, vx, sx, lb + w * 4096); dma16<3072>(rx, vx, sx, lb + w * 4096);
      const unsigned kt = (unsigned)t * 64 * C::LDW * 2;
#pragma unroll
      for (int i = 0; i < 3; ++i)
#pragma unroll
        for (int j = 0; j < 2; ++j)
          dma16<0>(rw, vw, sw0 + kt + (unsigned)(i * segstride + j * 8 * C::LDW) * 2, lb + C::XB + i * 8192 + (2 * w + j) * 1024);
      if (MODE == 1) dma16<0>(rw, ve, se0 + kt, lb + C::XB + C::WB + w * 1024);
    };
    issue(0, 0); issue(1, 1); issue(2, 2);
    wait_vm<2 * C::NLD>(); bar();
    int slot = 0;
    for (int t = 1; t < C::NT - 2; ++t) {
      wait_vm<C::NLD>(); bar();
      issue(t + 2, slot); slot = slot == 2 ? 0 : slot + 1;
    }
    wait_vm<C::NLD>(); bar();
    wait_vm<0>(); bar();
    return;
  }
  const int wm = wid >> 1, wn = wid & 1, r32 = lane & 31, h = lane >> 5;
  const int ta = 2 * wm + wn, tb = 2 * wm + (1 - wn);
  {
    const int c = threadIdx.x;
    int col = c < 192 ? seg0 + (c >> 6) * segstride + (c & 63) : ecol + (c & 31);
    const float s = cs[col] + cs[C::LDW + col];
    const float b = MODE == 1 ? bflo(bias[col]) : 0.f;
    *(LAS f32x2*)(lds + C::TAB + c * 8) = f32x2{s, b};
  }
  float mean[2], rstd[2];
#pragma unroll
  for (int a = 0; a < 2; ++a) {
    float var;
    load_stats(st, (mt * 4 + (a ? tb : ta)) * 32 + r32, h, mean[a], var);
    float r = __builtin_amdgcn_rsqf(var + EPS);
    if (MODE == 1) r *= __builtin_amdgcn_rsqf(var * r * r + EPS);
    rstd[a] = r;
    if (MODE == 0 && h == 0) *(LAS f32x2*)(lds + C::ROWT + wid * 512 + (a * 32 + r32) * 8) = f32x2{mean[a] * r, r};
  }
  f32x16 acc[2][3], acce;
#pragma unroll
  for (int a = 0; a < 2; ++a)
#pragma unroll
    for (int i = 0; i < 3; ++i) acc[a][i] = f32x16{};
  acce = f32x16{};
  const int xoa = ta * 4096 + lane * 16, xob = tb * 4096 + lane * 16;
  const int trl = ((lane >> 2) & 3) * 64 + ((lane >> 4) & 1) * 32 + (lane & 3) * 8;
  const int wo = C::XB + wn * 256 + h * 512 + trl, eo = C::XB + C::WB + h * 256 + trl;
  bf16x8 fx[2][2], fw[2][3], fe[2];
  auto rd = [&](int buf, lds_cptr sl, int s) {
    fx[buf][0] = lds_frag(sl + xoa + s * 1024);
    fx[buf][1] = lds_frag(sl + xob + s * 1024);
#pragma unroll
    for (int i = 0; i < 3; ++i) fw[buf][i] = cat8(lds_tr(sl + wo + i * 8192 + s * 2048), lds_tr(sl + wo + i * 8192 + s * 2048 + 1024));
    if (MODE == 1) fe[buf] = cat8(lds_tr(sl + eo + s * 1024), lds_tr(sl + eo + s * 1024 + 512));
  };
  auto mm = [&](int buf) {
#pragma unroll
    for (int i = 0; i < 3; ++i)
#pragma unroll
      for (int a = 0; a < 2; ++a) {
        if (MODE == 0 && i == 2) acc[a][i] = MFMA32(fx[buf][a], fw[buf][i], acc[a][i]);
        else acc[a][i] = MFMA32(fw[buf][i], fx[buf][a], acc[a][i]);
      }
    if (MODE == 1) acce = MFMA32(fe[buf], fx[buf][0], acce);
  };
  bar_lgkm();
  int slot = 0;
  rd(0, lds, 0);
  for (int t = 0; t < C::NT; ++t) {
    const lds_cptr sl = lds + slot * C::SLOT;
    slot = slot == 2 ? 0 : slot + 1;
    rd(1, sl, 1); mm(0); SBAR();
    rd(0, sl, 2); mm(1); SBAR();
    rd(1, sl, 3); mm(0); SBAR();
    if (t + 1 < C::NT) { bar_lgkm(); SBAR(); rd(0, lds + slot * C::SLOT, 0); }
    mm(1); SBAR();
  }
  const lds_cptr tab = lds + C::TAB;
  if (MODE == 0) {
    const int head = nt * 2 + wn;
#pragma unroll
    for (int a = 0; a < 2; ++a) {
      const int tt = mt * 4 + (a ? tb : ta);
#pragma unroll
      for (int i = 0; i < 2; ++i) {
        const float sc = i == 0 ? rstd[a] * QSCALE : rstd[a];
        f32x16 v;
#pragma unroll
        for (int g = 0; g < 4; ++g) {
          const f32x4 c0 = *(const LAS f32x4*)(tab + (i * 64 + wn * 32 + g * 8 + h * 4) * 8), c1 = *(const LAS f32x4*)(tab + (i * 64 + wn * 32 + g * 8 + h * 4) * 8 + 16);
          v[4 * g + 0] = (acc[a][i][4 * g + 0] - mean[a] * c0[0]) * sc; v[4 * g + 1] = (acc[a][i][4 * g + 1] - mean[a] * c0[2]) * sc;
          v[4 * g + 2] = (acc[a][i][4 * g + 2] - mean[a] * c1[0]) * sc; v[4 * g + 3] = (acc[a][i][4 * g + 3] - mean[a] * c1[2]) * sc;
        }
        u16* dst = i == 0 ? out0 + ((size_t)(head * 64 + tt) * 2 * 64 + lane) * 8 : out1 + ((size_t)(head * 64 + tt) * 4 * 64 + lane) * 8;
        *(u32x4*)dst = pack8(v, 0); *(u32x4*)(dst + 512) = pack8(v, 1);
      }
      {
        const float csv = *(const LAS float*)(tab + (128 + wn * 32 + r32) * 8);
        const lds_cptr rt = lds + C::ROWT + wid * 512 + a * 256;
        f32x16 v;
#pragma unroll
        for (int g = 0; g < 4; ++g) {
          const f32x4 c0 = *(const LAS f32x4*)(rt + (g * 8 + h * 4) * 8), c1 = *(const LAS f32x4*)(rt + (g * 8 + h * 4) * 8 + 16);
          v[4 * g + 0] = acc[a][2][4 * g + 0] * c0[1] - c0[0] * csv; v[4 * g + 1] = acc[a][2][4 * g + 1] * c0[3] - c0[2] * csv;
          v[4 * g + 2] = acc[a][2][4 * g + 2] * c1[1] - c1[0] * csv; v[4 * g + 3] = acc[a][2][4 * g + 3] * c1[3] - c1[2] * csv;
        }
        u16* dst = out1 + (((size_t)(head * 64 + tt) * 4 + 2) * 64 + lane) * 8;
        *(u32x4*)dst = pack8(v, 0); *(u32x4*)(dst + 512) = pack8(v, 1);
      }
    }
  } else {
    auto fin = [&](const f32x16& ac, int a, int tc  , int gcol  ) {
      const int tt = mt * 4 + (a ? tb : ta);
      const float mu = mean[a], r = rstd[a];
      f32x16 v;
#pragma unroll
      for (int g = 0; g < 4; ++g) {
        const f32x4 c0 = *(const LAS f32x4*)(tab + (tc + g * 8 + h * 4) * 8), c1 = *(const LAS f32x4*)(tab + (tc + g * 8 + h * 4) * 8 + 16);
        v[4 * g + 0] = (ac[4 * g + 0] - mu * c0[0]) * r + c0[1]; v[4 * g + 1] = (ac[4 * g + 1] - mu * c0[2]) * r + c0[3];
        v[4 * g + 2] = (ac[4 * g + 2] - mu * c1[0]) * r + c1[1]; v[4 * g + 3] = (ac[4 * g + 3] - mu * c1[2]) * r + c1[3];
      }
#pragma unroll
      for (int e = 0; e < 16; ++e) {
        const float z = v[e];
        const float q = z * (-2.302208198f - 0.1029432397f * z * z);
        v[e] = z * __builtin_amdgcn_rcpf(1.0f + __builtin_amdgcn_exp2f(q));
      }
      u16* dst = out0 + (((size_t)tt * (F_ / 16) + (gcol >> 4)) * 64 + lane) * 8;
      *(u32x4*)dst = pack8(v, 0); *(u32x4*)(dst + 512) = pack8(v, 1);
    };
#pragma unroll
    for (int a = 0; a < 2; ++a)
#pragma unroll
      for (int i = 0; i < 3; ++i) fin(acc[a][i], a, i * 64 + wn * 32, seg0 + i * 64 + wn * 32);
    fin(acce, 0, 192, ecol);
  }
}

template <int KD> struct Small {
  static constexpr int XB = 16384, WB = 8192, SLOT = XB + WB, NS = 6, NT = KD / 64, NLD = 6, LDS = NS * SLOT;
};
template <int N> DI void small_tail_wait() { wait_vm<N * 6>(); bar(); }

template <int KD, bool BIAS, bool FINAL>
__global__ __launch_bounds__(512, 2) void gemm_small(const u16* __restrict__ af, const u16* __restrict__ wmat, const u16* __restrict__ bias,
                                                     const u16* __restrict__ resid, u16* __restrict__ out, f32x2* __restrict__ st) {
  using C = Small<KD>;
  __shared__ __attribute__((aligned(16))) char lds_[C::LDS];
  const lds_ptr lds = (lds_ptr)lds_;
  const unsigned lds0 = (unsigned)(size_t)lds;
  const int lane = threadIdx.x & 63, wid = __builtin_amdgcn_readfirstlane(threadIdx.x >> 6);
  const int xcd = blockIdx.x & 7, idx = blockIdx.x >> 3;
  const int mt = (xcd & 3) * 4 + (idx & 3), nt = (xcd >> 2) * 8 + (idx >> 2);
  if (wid >= 4) {
    const int w = wid - 4;
    const __amdgpu_buffer_rsrc_t rx = mkrsrc(af, S_ * KD * 2), rw = mkrsrc(wmat, KD * D_ * 2);
    const unsigned vx = lane * 16;
    const unsigned vw = (((lane >> 5) * 4 + ((lane >> 2) & 3)) * D_ + ((lane >> 4) & 1) * 32 + (lane & 3) * 8) * 2;
    const unsigned sx0 = (unsigned)(mt * 4 + w) * (KD / 16) * 1024;
    const unsigned sw0 = (unsigned)(w * 16 * D_ + nt * 64) * 2;
    auto issue = [&](int t, int slot) {
      const unsigned lb = lds0 + slot * C::SLOT;
      const unsigned sx = sx0 + t * 4096;
      dma16<0>(rx, vx, sx, lb + w * 4096); dma16<1024>(rx, vx, sx, lb + w * 4096);
      dma16<2048>(rx, vx, sx, lb + w * 4096); dma16<3072>(rx, vx, sx, lb + w * 4096);
      const unsigned kt = (unsigned)t * 64 * D_ * 2;
      dma16<0>(rw, vw, sw0 + kt, lb + C::XB + (2 * w) * 1024);
      dma16<0>(rw, vw, sw0 + kt + 8 * D_ * 2, lb + C::XB + (2 * w + 1) * 1024);
    };
#pragma unroll
    for (int t = 0; t < C::NS; ++t) issue(t, t);
    wait_vm<(C::NS - 1) * C::NLD>(); bar();
    int slot = 0;
    for (int t = 1; t < C::NT - (C::NS - 1); ++t) {
      wait_vm<(C::NS - 2) * C::NLD>(); bar();
      issue(t + C::NS - 1, slot); slot = slot == C::NS - 1 ? 0 : slot + 1;
    }
    small_tail_wait<4>(); small_tail_wait<3>(); small_tail_wait<2>(); small_tail_wait<1>(); small_tail_wait<0>();
    return;
  }
  const int r32 = lane & 31, h = lane >> 5, tt = mt * 4 + wid;
  u32x4 res[4];
  u32x2 bs[8];
#pragma unroll
  for (int s = 0; s < 4; ++s) res[s] = *(const u32x4*)(resid + (((size_t)tt * 64 + nt * 4 + s) * 64 + lane) * 8);
  if (BIAS)
#pragma unroll
    for (int g = 0; g < 8; ++g) bs[g] = *(const u32x2*)(bias + nt * 64 + g * 8 + h * 4);
  f32x16 acc[2];
  acc[0] = f32x16{}; acc[1] = f32x16{};
  const int xo = wid * 4096 + lane * 16;
  const int wo = C::XB + h * 512 + ((lane >> 2) & 3) * 64 + ((lane >> 4) & 1) * 32 + (lane & 3) * 8;
  bf16x8 fx[2], fw[2][2];
  auto rd = [&](int buf, lds_cptr sl, int s) {
    fx[buf] = lds_frag(sl + xo + s * 1024);
#pragma unroll
    for (int i = 0; i < 2; ++i) fw[buf][i] = cat8(lds_tr(sl + wo + i * 256 + s * 2048), lds_tr(sl + wo + i * 256 + s * 2048 + 1024));
  };
  auto mm = [&](int buf) {
    acc[0] = MFMA32(fw[buf][0], fx[buf], acc[0]);
    acc[1] = MFMA32(fw[buf][1], fx[buf], acc[1]);
  };
  bar();
  int slot = 0;
  rd(0, lds, 0);
  for (int t = 0; t < C::NT; ++t) {
    const lds_cptr sl = lds + slot * C::SLOT;
    slot = slot == C::NS - 1 ? 0 : slot + 1;
    rd(1, sl, 1); mm(0); SBAR();
    rd(0, sl, 2); mm(1); SBAR();
    rd(1, sl, 3); mm(0); SBAR();
    if (t + 1 < C::NT) { bar_lgkm(); SBAR(); rd(0, lds + slot * C::SLOT, 0); }
    mm(1); SBAR();
  }
  float s1 = 0.f, s2 = 0.f;
#pragma unroll
  for (int s = 0; s < 4; ++s) {
    const f32x16& ac = acc[s >> 1];
    u32x4 o;
#pragma unroll
    for (int i = 0; i < 4; ++i) {
      const int e = (s & 1) * 8 + 2 * i;
      float v0 = ac[e], v1 = ac[e + 1];
      if (BIAS) { const unsigned bb = bs[s * 2 + (i >> 1)][i & 1]; v0 += bflo(bb); v1 += bfhi(bb); }
      v0 += bflo(res[s][i]); v1 += bfhi(res[s][i]);
      o[i] = cvtpk(v0, v1);
      if (!FINAL) { const float a = bflo(o[i]), c = bfhi(o[i]); s1 += a + c; s2 += a * a + c * c; }
    }
    if (FINAL) *(u32x4*)(out + (size_t)(tt * 32 + r32) * D_ + nt * 64 + s * 16 + h * 8) = nat2frag(o);
    else *(u32x4*)(out + (((size_t)tt * 64 + nt * 4 + s) * 64 + lane) * 8) = o;
  }
  if (!FINAL) {
    s1 = swap_add(s1); s2 = swap_add(s2);
    if (h == 0) st[nt * S_ + tt * 32 + r32] = f32x2{s1, s2};
  }
}

__global__ __launch_bounds__(512, 2) void attn_kernel(const u16* __restrict__ qf, const u16* __restrict__ kvf, u16* __restrict__ ao) {
  __shared__ __attribute__((aligned(16))) float xch[8][17][64];
  const int lane = threadIdx.x & 63, wid = __builtin_amdgcn_readfirstlane(threadIdx.x >> 6);
  const int xcd = blockIdx.x & 7, idx = blockIdx.x >> 3;
  const int head = xcd * 4 + (idx >> 3), qb = idx & 7;
  const int qt0 = qb * 8 + (wid & 3) * 2, kh = wid >> 2;
  const bf16x8* qp = (const bf16x8*)(qf + (size_t)(head * 64 + qt0) * 1024) + lane;
  const bf16x8* kv0 = (const bf16x8*)(kvf + (size_t)head * 64 * 2048) + lane;
  bf16x8 q[2][2];
#pragma unroll
  for (int a = 0; a < 2; ++a) { q[a][0] = qp[a * 128]; q[a][1] = qp[a * 128 + 64]; }
  f32x16 negm[2];
  float mref[2];
  {
    const bf16x8 k0 = kv0[0], k1 = kv0[64];
#pragma unroll
    for (int a = 0; a < 2; ++a) {
      f32x16 s = MFMA32(k0, q[a][0], f32x16{});
      s = MFMA32(k1, q[a][1], s);
      float m = s[0];
#pragma unroll
      for (int e = 1; e < 16; ++e) m = __builtin_fmaxf(m, s[e]);
      m = swap_max(m);
      mref[a] = m;
#pragma unroll
      for (int e = 0; e < 16; ++e) negm[a][e] = -m;
    }
  }
  f32x16 o[2];
  o[0] = f32x16{}; o[1] = f32x16{};
  f32x4 ls[2];
  ls[0] = f32x4{}; ls[1] = f32x4{};
  bf16x8 sel;
  { const short one = (lane == 0 || lane == 32 || lane == 17 || lane == 49) ? (short)0x3f80 : (short)0;
    sel = bf16x8{one, one, one, one, one, one, one, one}; }
  const bf16x8* kvp = kv0 + kh * 32 * 256;
  bf16x8 kc[2], vc[2], kn[2], vn[2];
  kc[0] = kvp[0]; kc[1] = kvp[64]; vc[0] = kvp[128]; vc[1] = kvp[192];
  kn[0] = kvp[256]; kn[1] = kvp[256 + 64]; vn[0] = kvp[256 + 128]; vn[1] = kvp[256 + 192];
  for (int j = 0; j < 32; ++j) {
    const int jn = j + 2 < 32 ? j + 2 : 31;
    bf16x8 k2[2], v2[2];
    k2[0] = kvp[jn * 256]; k2[1] = kvp[jn * 256 + 64]; v2[0] = kvp[jn * 256 + 128]; v2[1] = kvp[jn * 256 + 192];
#pragma unroll
    for (int a = 0; a < 2; ++a) {
      f32x16 s = MFMA32(kc[0], q[a][0], negm[a]);
      s = MFMA32(kc[1], q[a][1], s);
#pragma unroll
      for (int e = 0; e < 16; ++e) s[e] = __builtin_amdgcn_exp2f(s[e]);
      const bf16x8 p0 = __builtin_bit_cast(bf16x8, pack8(s, 0)), p1 = __builtin_bit_cast(bf16x8, pack8(s, 1));
      o[a] = MFMA32(vc[0], p0, o[a]);
      o[a] = MFMA32(vc[1], p1, o[a]);
      ls[a] = MFMA16(sel, p0, ls[a]);
      ls[a] = MFMA16(sel, p1, ls[a]);
    }
    kc[0] = kn[0]; kc[1] = kn[1]; vc[0] = vn[0]; vc[1] = vn[1];
    kn[0] = k2[0]; kn[1] = k2[1]; vn[0] = v2[0]; vn[1] = v2[1];
  }
  float l[2];
#pragma unroll
  for (int a = 0; a < 2; ++a) {
    const float r0 = __shfl(ls[a][0], lane & 15), r1 = __shfl(ls[a][1], lane & 15);
    l[a] = (lane & 16) ? r1 : r0;
  }
  {
    const f32x16& og = kh ? o[0] : o[1];
#pragma unroll
    for (int e = 0; e < 16; ++e) xch[wid][e][lane] = og[e];
    xch[wid][16][lane] = kh ? l[0] : l[1];
  }
  __syncthreads();
  f32x16 of = kh ? o[1] : o[0];
  float lf = kh ? l[1] : l[0];
#pragma unroll
  for (int e = 0; e < 16; ++e) of[e] += xch[wid ^ 4][e][lane];
  lf += xch[wid ^ 4][16][lane];
  float chk = 0.f;
#pragma unroll
  for (int e = 0; e < 16; ++e) chk = __builtin_fmaxf(chk, __builtin_fabsf(of[e]));
  const bool bad = !(lf < 1.0e18f) || !(chk < 1.0e36f);
  if (__builtin_expect(__any(bad), 0)) {
    const bf16x8 qa = kh ? q[1][0] : q[0][0], qc = kh ? q[1][1] : q[0][1];
    float m = kh ? mref[1] : mref[0];
    of = f32x16{}; lf = 0.f;
    for (int j = 0; j < 64; ++j) {
      f32x16 s = MFMA32(kv0[j * 256], qa, f32x16{});
      s = MFMA32(kv0[j * 256 + 64], qc, s);
      float mx = s[0];
#pragma unroll
      for (int e = 1; e < 16; ++e) mx = __builtin_fmaxf(mx, s[e]);
      mx = __builtin_fmaxf(swap_max(mx), m);
      const float f = __builtin_amdgcn_exp2f(m - mx);
      m = mx;
      float ps = 0.f;
#pragma unroll
      for (int e = 0; e < 16; ++e) { s[e] = __builtin_amdgcn_exp2f(s[e] - m); ps += s[e]; of[e] *= f; }
      lf = lf * f + swap_add(ps);
      of = MFMA32(kv0[j * 256 + 128], __builtin_bit_cast(bf16x8, pack8(s, 0)), of);
      of = MFMA32(kv0[j * 256 + 192], __builtin_bit_cast(bf16x8, pack8(s, 1)), of);
    }
  }
  const float rl = 1.0f / lf;
#pragma unroll
  for (int e = 0; e < 16; ++e) of[e] *= rl;
  u16* dst = ao + (((size_t)(qt0 + kh) * 64 + head * 2) * 64 + lane) * 8;
  *(u32x4*)dst = pack8(of, 0);
  *(u32x4*)(dst + 512) = pack8(of, 1);
}

}

extern "C" void kernel_launch(void* const* d_in, const int* in_sizes, int n_in, void* d_out, int out_size, void* d_ws, size_t ws_size,
                              hipStream_t stream) {
  const u16* x = (const u16*)d_in[0];
  const u16* wqkv = (const u16*)d_in[1];
  const u16* wo = (const u16*)d_in[2];
  const u16* w1 = (const u16*)d_in[4];
  const u16* b1 = (const u16*)d_in[5];
  const u16* w2 = (const u16*)d_in[6];
  const u16* b2 = (const u16*)d_in[7];
  char* ws = (char*)d_ws;
  u16* X[4] = {(u16*)(ws + WS_X0), (u16*)(ws + WS_X1), (u16*)(ws + WS_X2), (u16*)(ws + WS_X3)};
  f32x2* ST[4] = {(f32x2*)(ws + WS_ST0), (f32x2*)(ws + WS_ST1), (f32x2*)(ws + WS_ST2), (f32x2*)(ws + WS_ST3)};
  u16 *Q = (u16*)(ws + WS_Q), *KV = (u16*)(ws + WS_KV), *AO = (u16*)(ws + WS_AO), *Hb = (u16*)(ws + WS_H);
  float *csq = (float*)(ws + WS_CSQ), *cs1 = (float*)(ws + WS_CS1);
  prep_kernel<<<336, 512, 0, stream>>>(x, wqkv, w1, X[0], ST[0], csq, cs1);
  for (int l = 0; l < 2; ++l) {
    gemm_big<0><<<256, 512, 0, stream>>>(X[2 * l], wqkv, ST[2 * l], csq, b1, Q, KV);
    attn_kernel<<<256, 512, 0, stream>>>(Q, KV, AO);
    gemm_small<1024, false, false><<<256, 512, 0, stream>>>(AO, wo, b2, X[2 * l], X[2 * l + 1], ST[2 * l + 1]);
    gemm_big<1><<<256, 512, 0, stream>>>(X[2 * l + 1], w1, ST[2 * l + 1], cs1, b1, Hb, Hb);
    if (l == 0) gemm_small<F_, true, false><<<256, 512, 0, stream>>>(Hb, w2, b2, X[1], X[2], ST[2]);
    else gemm_small<F_, true, true><<<256, 512, 0, stream>>>(Hb, w2, b2, X[3], (u16*)d_out, ST[3]);
  }
}
```

```cpp
#include <hip/hip_runtime.h>
#include <hip/hip_bf16.h>

#define KID_ARG
#define KID(x)
#define STAMP0
#define STAMPT(i)
#define STAMPE(i)

namespace {

typedef short bf16x8 __attribute__((ext_vector_type(8)));
typedef short s16x4 __attribute__((ext_vector_type(4)));
typedef float f32x16 __attribute__((ext_vector_type(16)));
typedef float f32x4 __attribute__((ext_vector_type(4)));
typedef float f32x2 __attribute__((ext_vector_type(2)));
typedef unsigned u32x4 __attribute__((ext_vector_type(4)));
typedef unsigned u32x2 __attribute__((ext_vector_type(2)));
typedef __bf16 bf16x2_t __attribute__((ext_vector_type(2)));
typedef unsigned short u16;
#define LAS __attribute__((address_space(3)))
typedef LAS const char* lds_cptr;
typedef LAS char* lds_ptr;
#define DI __device__ __forceinline__
#define MFMA32(a, b, c) __builtin_amdgcn_mfma_f32_32x32x16_bf16((a), (b), (c), 0, 0, 0)
#define MFMA16(a, b, c) __builtin_amdgcn_mfma_f32_16x16x32_bf16((a), (b), (c), 0, 0, 0)
#define SBAR() __builtin_amdgcn_sched_barrier(0)

constexpr int S_ = 2048, D_ = 1024, H_ = 32, F_ = 3584;
constexpr float EPS = 1e-5f;
constexpr float QSCALE = 0.17677669529663687f * 1.4426950408889634f;

constexpr size_t MB = 1u << 20;
constexpr size_t WS_X0 = 0, WS_X1 = 4 * MB, WS_X2 = 8 * MB, WS_X3 = 12 * MB;
constexpr size_t WS_Q = 16 * MB, WS_KV = 20 * MB, WS_AO = 28 * MB, WS_H = 32 * MB;
constexpr size_t WS_ST0 = 48 * MB, WS_ST1 = WS_ST0 + 256 * 1024, WS_ST2 = WS_ST1 + 256 * 1024, WS_ST3 = WS_ST2 + 256 * 1024;
constexpr size_t WS_CSQ = 49 * MB, WS_CS1 = WS_CSQ + 2 * 3072 * 4;

DI unsigned cvtpk(float lo, float hi) { f32x2 v = {lo, hi}; return __builtin_bit_cast(unsigned, __builtin_convertvector(v, bf16x2_t)); }
DI float bflo(unsigned u) { return __uint_as_float(u << 16); }
DI float bfhi(unsigned u) { return __uint_as_float(u & 0xffff0000u); }
DI float swap_add(float v) {
  auto r = __builtin_amdgcn_permlane32_swap(__float_as_uint(v), __float_as_uint(v), false, false);
  return __uint_as_float(r[0]) + __uint_as_float(r[1]);
}
DI float swap_max(float v) {
  auto r = __builtin_amdgcn_permlane32_swap(__float_as_uint(v), __float_as_uint(v), false, false);
  return __builtin_fmaxf(__uint_as_float(r[0]), __uint_as_float(r[1]));
}
DI u32x4 pack8(const f32x16& x, int s) {
  return u32x4{cvtpk(x[8 * s], x[8 * s + 1]), cvtpk(x[8 * s + 2], x[8 * s + 3]), cvtpk(x[8 * s + 4], x[8 * s + 5]), cvtpk(x[8 * s + 6], x[8 * s + 7])};
}
DI u32x4 nat2frag(u32x4 v) {
  auto r0 = __builtin_amdgcn_permlane32_swap(v[0], v[2], false, false);
  auto r1 = __builtin_amdgcn_permlane32_swap(v[1], v[3], false, false);
  return u32x4{r0[0], r1[0], r0[1], r1[1]};
}
DI bf16x8 lds_frag(lds_cptr p) { return *(const LAS bf16x8*)p; }
DI s16x4 lds_tr(lds_cptr p) { return __builtin_bit_cast(s16x4, __builtin_amdgcn_ds_read_tr16_b64_v4i16((LAS s16x4*)p)); }
DI bf16x8 cat8(s16x4 a, s16x4 b) { return __builtin_shufflevector(a, b, 0, 1, 2, 3, 4, 5, 6, 7); }
template <int N> DI void wait_vm() { asm volatile("s_waitcnt vmcnt(%0)" ::"i"(N) : "memory"); }
DI void bar() { asm volatile("s_barrier" ::: "memory"); }
DI void bar_lgkm() { asm volatile("s_waitcnt lgkmcnt(0)\n\ts_barrier" ::: "memory"); }
template <int IMM> DI void dma16(__amdgpu_buffer_rsrc_t rs, unsigned voff, unsigned soff, unsigned ldsaddr) {
  asm volatile("s_mov_b32 m0, %0\n\ts_nop 0\n\tbuffer_load_dwordx4 %1, %2, %3 offen offset:%4 lds" ::"s"(ldsaddr), "v"(voff), "s"(rs), "s"(soff), "i"(IMM) : "memory");
}
DI __amdgpu_buffer_rsrc_t mkrsrc(const void* p, unsigned bytes) { return __builtin_amdgcn_make_buffer_rsrc((void*)p, 0, bytes, 0x00020000); }

DI void load_stats(const f32x2* st, int token, int h, float& mean, float& var) {
  float s1 = 0.f, s2 = 0.f;
#pragma unroll
  for (int p = 0; p < 8; ++p) { const f32x2 v = st[(8 * h + p) * S_ + token]; s1 += v.x; s2 += v.y; }
  s1 = swap_add(s1); s2 = swap_add(s2);
  mean = s1 * (1.0f / D_);
  var = __builtin_fmaxf(s2 * (1.0f / D_) - mean * mean, 0.f);
}

__global__ __launch_bounds__(512) void prep_kernel(const u16* __restrict__ x, const u16* __restrict__ wqkv, const u16* __restrict__ w1,
                                                   u16* __restrict__ xf, f32x2* __restrict__ st, float* __restrict__ csq, float* __restrict__ cs1 KID_ARG) {
  STAMP0;
  __shared__ float red[8][64];
  const int lane = threadIdx.x & 63, w = threadIdx.x >> 6, b = blockIdx.x;
  if (b < 128) {
    const int tt = b >> 1, p = (b & 1) * 8 + w, t = lane & 31, h = lane >> 5;
    const u16* src = x + (size_t)(tt * 32 + t) * D_ + p * 64 + h * 8;
    u32x4 v[4];
#pragma unroll
    for (int s = 0; s < 4; ++s) v[s] = *(const u32x4*)(src + s * 16);
    float s1 = 0.f, s2 = 0.f;
#pragma unroll
    for (int s = 0; s < 4; ++s) {
#pragma unroll
      for (int i = 0; i < 4; ++i) { const float a = bflo(v[s][i]), c = bfhi(v[s][i]); s1 += a + c; s2 += a * a + c * c; }
      *(u32x4*)(xf + ((size_t)(tt * 64 + p * 4 + s) * 64 + lane) * 8) = nat2frag(v[s]);
    }
    s1 = swap_add(s1); s2 = swap_add(s2);
    if (h == 0) st[p * S_ + tt * 32 + t] = f32x2{s1, s2};
  } else {
    const bool isq = b < 224;
    const int bb = isq ? b - 128 : b - 224, strip = bb >> 1, half = bb & 1, ld = isq ? 3072 : F_;
    const u16* src = (isq ? wqkv : w1) + (size_t)(half * 512 + (threadIdx.x >> 3) * 8) * ld + strip * 64 + (lane & 7) * 8;
    u32x4 v[8];
#pragma unroll
    for (int r = 0; r < 8; ++r) v[r] = *(const u32x4*)(src + (size_t)r * ld);
    float a[8];
#pragma unroll
    for (int i = 0; i < 8; ++i) a[i] = 0.f;
#pragma unroll
    for (int r = 0; r < 8; ++r)
#pragma unroll
      for (int i = 0; i < 4; ++i) { a[2 * i] += bflo(v[r][i]); a[2 * i + 1] += bfhi(v[r][i]); }
#pragma unroll
    for (int i = 0; i < 8; ++i) { a[i] += __shfl_xor(a[i], 8); a[i] += __shfl_xor(a[i], 16); a[i] += __shfl_xor(a[i], 32); }
    if (lane < 8)
#pragma unroll
      for (int i = 0; i < 8; ++i) red[w][lane * 8 + i] = a[i];
    __syncthreads();
    if (threadIdx.x < 64) {
      float s = 0.f;
#pragma unroll
      for (int i = 0; i < 8; ++i) s += red[i][threadIdx.x];
      (isq ? csq : cs1)[half * ld + strip * 64 + threadIdx.x] = s;
    }
  }
  STAMPT(1); STAMPT(2); STAMPE(3);
}

template <int MODE  > struct Big {
  static constexpr int LDW = MODE == 0 ? 3072 : F_;
  static constexpr int XB = 16384, WB = 24576, EB = MODE == 1 ? 4096 : 0, SLOT = XB + WB + EB, NS = 3, NT = 16;
  static constexpr int NLD = 4 + 6 + (MODE == 1 ? 1 : 0);
  static constexpr int TAB = NS * SLOT;
  static constexpr int ROWT = TAB + 2048;
  static constexpr int LDS = ROWT + 4 * 512;
};

template <int MODE>
__global__ __launch_bounds__(512, 2) void gemm_big(const u16* __restrict__ xf, const u16* __restrict__ wmat, const f32x2* __restrict__ st,
                                                   const float* __restrict__ cs, const u16* __restrict__ bias, u16* __restrict__ out0, u16* __restrict__ out1 KID_ARG) {
  using C = Big<MODE>;
  STAMP0;
  __shared__ __attribute__((aligned(16))) char lds_[C::LDS];
  const lds_ptr lds = (lds_ptr)lds_;
  const unsigned lds0 = (unsigned)(size_t)lds;
  const int lane = threadIdx.x & 63, wid = __builtin_amdgcn_readfirstlane(threadIdx.x >> 6);
  const int xcd = blockIdx.x & 7, idx = blockIdx.x >> 3;
  const int mt = (xcd & 1) * 8 + (idx & 7), nt = (xcd >> 1) * 4 + (idx >> 3);
  int seg0, segstride, ecol;
  if (MODE == 0) { seg0 = nt * 64; segstride = 1024; ecol = 0; }
  else { seg0 = (nt >> 1) * 448 + (nt & 1) * 256; segstride = 64; ecol = (nt >> 1) * 448 + 192 + (nt & 1) * 32; }

  if (wid >= 4) {
    const int w = wid - 4;
    const __amdgpu_buffer_rsrc_t rx = mkrsrc(xf, S_ * D_ * 2), rw = mkrsrc(wmat, 1024u * C::LDW * 2);
    const unsigned vx = lane * 16;
    const unsigned vw = (((lane >> 5) * 4 + ((lane >> 2) & 3)) * C::LDW + ((lane >> 4) & 1) * 32 + (lane & 3) * 8) * 2;
    const unsigned ve = (((lane >> 4) * 4 + ((lane >> 2) & 3)) * C::LDW + (lane & 3) * 8) * 2;
    const unsigned sx0 = (unsigned)(mt * 4 + w) * 64 * 1024;
    const unsigned sw0 = (unsigned)(w * 16 * C::LDW + seg0) * 2, se0 = (unsigned)(w * 16 * C::LDW + ecol) * 2;
    auto issue = [&](int t, int slot) {
      const unsigned lb = lds0 + slot * C::SLOT;
      const unsigned sx = sx0 + t * 4096;
      dma16<0>(rx, vx, sx, lb + w * 4096); dma16<1024>(rx, vx, sx, lb + w * 4096);
      dma16<2048>(rx, vx, sx, lb + w * 4096); dma16<3072>(rx, vx, sx, lb + w * 4096);
      const unsigned kt = (unsigned)t * 64 * C::LDW * 2;
#pragma unroll
      for (int i = 0; i < 3; ++i)
#pragma unroll
        for (int j = 0; j < 2; ++j)
          dma16<0>(rw, vw, sw0 + kt + (unsigned)(i * segstride + j * 8 * C::LDW) * 2, lb + C::XB + i * 8192 + (2 * w + j) * 1024);
      if (MODE == 1) dma16<0>(rw, ve, se0 + kt, lb + C::XB + C::WB + w * 1024);
    };
    issue(0, 0); issue(1, 1); issue(2, 2);
    wait_vm<2 * C::NLD>(); bar();
    int slot = 0;
    for (int t = 1; t < C::NT - 2; ++t) {
      wait_vm<C::NLD>(); bar();
      issue(t + 2, slot); slot = slot == 2 ? 0 : slot + 1;
    }
    wait_vm<C::NLD>(); bar();
    wait_vm<0>(); bar();
    return;
  }
  const int wm = wid >> 1, wn = wid & 1, r32 = lane & 31, h = lane >> 5;
  const int ta = 2 * wm + wn, tb = 2 * wm + (1 - wn);
  {
    const int c = threadIdx.x;
    int col = c < 192 ? seg0 + (c >> 6) * segstride + (c & 63) : ecol + (c & 31);
    const float s = cs[col] + cs[C::LDW + col];
    const float b = MODE == 1 ? bflo(bias[col]) : 0.f;
    *(LAS f32x2*)(lds + C::TAB + c * 8) = f32x2{s, b};
  }
  float mean[2], rstd[2];
#pragma unroll
  for (int a = 0; a < 2; ++a) {
    float var;
    load_stats(st, (mt * 4 + (a ? tb : ta)) * 32 + r32, h, mean[a], var);
    float r = __builtin_amdgcn_rsqf(var + EPS);
    if (MODE == 1) r *= __builtin_amdgcn_rsqf(var * r * r + EPS);
    rstd[a] = r;
    if (MODE == 0 && h == 0) *(LAS f32x2*)(lds + C::ROWT + wid * 512 + (a * 32 + r32) * 8) = f32x2{mean[a] * r, r};
  }
  f32x16 acc[2][3], acce;
#pragma unroll
  for (int a = 0; a < 2; ++a)
#pragma unroll
    for (int i = 0; i < 3; ++i) acc[a][i] = f32x16{};
  acce = f32x16{};
  const int xoa = ta * 4096 + lane * 16, xob = tb * 4096 + lane * 16;
  const int trl = ((lane >> 2) & 3) * 64 + ((lane >> 4) & 1) * 32 + (lane & 3) * 8;
  const int wo = C::XB + wn * 256 + h * 512 + trl, eo = C::XB + C::WB + h * 256 + trl;
  bf16x8 fx[2][2], fw[2][3], fe[2];
  auto rdw = [&](lds_cptr sl, int i, int s) { return cat8(lds_tr(sl + wo + i * 8192 + s * 2048), lds_tr(sl + wo + i * 8192 + s * 2048 + 1024)); };
  auto mf = [&](int b, int i, int a) {
    if (MODE == 0 && i == 2) acc[a][i] = MFMA32(fx[b][a], fw[b][i], acc[a][i]);
    else acc[a][i] = MFMA32(fw[b][i], fx[b][a], acc[a][i]);
    SBAR();
  };
  auto step = [&](int b, lds_cptr sl, int s, bool BAR, bool RD) {
    const int n = b ^ 1;
    mf(b, 0, 0);
    if (BAR) { mf(b, 0, 1); bar_lgkm(); SBAR(); }
    if (RD) { fx[n][0] = lds_frag(sl + xoa + s * 1024); fw[n][0] = rdw(sl, 0, s); SBAR(); }
    if (!BAR) mf(b, 0, 1); else mf(b, 1, 0);
    if (RD) { fx[n][1] = lds_frag(sl + xob + s * 1024); fw[n][1] = rdw(sl, 1, s); SBAR(); }
    if (!BAR) mf(b, 1, 0); else mf(b, 1, 1);
    if (RD) { fw[n][2] = rdw(sl, 2, s); SBAR(); }
    if (!BAR) mf(b, 1, 1); else mf(b, 2, 0);
    if (RD && MODE == 1) { fe[n] = cat8(lds_tr(sl + eo + s * 1024), lds_tr(sl + eo + s * 1024 + 512)); SBAR(); }
    if (!BAR) mf(b, 2, 0);
    mf(b, 2, 1);
    if (MODE == 1) { acce = MFMA32(fe[b], fx[b][0], acce); SBAR(); }
  };
  bar_lgkm();
  STAMPT(1);
  int slot = 0;
  fx[0][0] = lds_frag(lds + xoa); fw[0][0] = rdw(lds, 0, 0); fx[0][1] = lds_frag(lds + xob); fw[0][1] = rdw(lds, 1, 0); fw[0][2] = rdw(lds, 2, 0);
  if (MODE == 1) fe[0] = cat8(lds_tr(lds + eo), lds_tr(lds + eo + 512));
  SBAR();
  for (int t = 0; t < C::NT - 1; ++t) {
    const lds_cptr sl = lds + slot * C::SLOT;
    slot = slot == 2 ? 0 : slot + 1;
    step(0, sl, 1, false, true); step(1, sl, 2, false, true); step(0, sl, 3, false, true);
    step(1, lds + slot * C::SLOT, 0, true, true);
  }
  {
    const lds_cptr sl = lds + slot * C::SLOT;
    step(0, sl, 1, false, true); step(1, sl, 2, false, true); step(0, sl, 3, false, true);
    step(1, sl, 0, false, false);
  }
  STAMPT(2);
  const lds_cptr tab = lds + C::TAB;
  if (MODE == 0) {
    const int head = nt * 2 + wn;
#pragma unroll
    for (int a = 0; a < 2; ++a) {
      const int tt = mt * 4 + (a ? tb : ta);
#pragma unroll
      for (int i = 0; i < 2; ++i) {
        const float sc = i == 0 ? rstd[a] * QSCALE : rstd[a];
        f32x16 v;
#pragma unroll
        for (int g = 0; g < 4; ++g) {
          const f32x4 c0 = *(const LAS f32x4*)(tab + (i * 64 + wn * 32 + g * 8 + h * 4) * 8), c1 = *(const LAS f32x4*)(tab + (i * 64 + wn * 32 + g * 8 + h * 4) * 8 + 16);
          v[4 * g + 0] = (acc[a][i][4 * g + 0] - mean[a] * c0[0]) * sc; v[4 * g + 1] = (acc[a][i][4 * g + 1] - mean[a] * c0[2]) * sc;
          v[4 * g + 2] = (acc[a][i][4 * g + 2] - mean[a] * c1[0]) * sc; v[4 * g + 3] = (acc[a][i][4 * g + 3] - mean[a] * c1[2]) * sc;
        }
        u16* dst = i == 0 ? out0 + ((size_t)(head * 64 + tt) * 2 * 64 + lane) * 8 : out1 + ((size_t)(head * 64 + tt) * 4 * 64 + lane) * 8;
        *(u32x4*)dst = pack8(v, 0); *(u32x4*)(dst + 512) = pack8(v, 1);
      }
      {
        const float csv = *(const LAS float*)(tab + (128 + wn * 32 + r32) * 8);
        const lds_cptr rt = lds + C::ROWT + wid * 512 + a * 256;
        f32x16 v;
#pragma unroll
        for (int g = 0; g < 4; ++g) {
          const f32x4 c0 = *(const LAS f32x4*)(rt + (g * 8 + h * 4) * 8), c1 = *(const LAS f32x4*)(rt + (g * 8 + h * 4) * 8 + 16);
          v[4 * g + 0] = acc[a][2][4 * g + 0] * c0[1] - c0[0] * csv; v[4 * g + 1] = acc[a][2][4 * g + 1] * c0[3] - c0[2] * csv;
          v[4 * g + 2] = acc[a][2][4 * g + 2] * c1[1] - c1[0] * csv; v[4 * g + 3] = acc[a][2][4 * g + 3] * c1[3] - c1[2] * csv;
        }
        u16* dst = out1 + (((size_t)(head * 64 + tt) * 4 + 2) * 64 + lane) * 8;
        *(u32x4*)dst = pack8(v, 0); *(u32x4*)(dst + 512) = pack8(v, 1);
      }
    }
  } else {
    auto fin = [&](const f32x16& ac, int a, int tc  , int gcol  ) {
      const int tt = mt * 4 + (a ? tb : ta);
      const float mu = mean[a], r = rstd[a];
      f32x16 v;
#pragma unroll
      for (int g = 0; g < 4; ++g) {
        const f32x4 c0 = *(const LAS f32x4*)(tab + (tc + g * 8 + h * 4) * 8), c1 = *(const LAS f32x4*)(tab + (tc + g * 8 + h * 4) * 8 + 16);
        v[4 * g + 0] = (ac[4 * g + 0] - mu * c0[0]) * r + c0[1]; v[4 * g + 1] = (ac[4 * g + 1] - mu * c0[2]) * r + c0[3];
        v[4 * g + 2] = (ac[4 * g + 2] - mu * c1[0]) * r + c1[1]; v[4 * g + 3] = (ac[4 * g + 3] - mu * c1[2]) * r + c1[3];
      }
#pragma unroll
      for (int e = 0; e < 16; ++e) {
        const float z = v[e];
        const float q = z * (-2.302208198f - 0.1029432397f * z * z);
        v[e] = z * __builtin_amdgcn_rcpf(1.0f + __builtin_amdgcn_exp2f(q));
      }
      u16* dst = out0 + (((size_t)tt * (F_ / 16) + (gcol >> 4)) * 64 + lane) * 8;
      *(u32x4*)dst = pack8(v, 0); *(u32x4*)(dst + 512) = pack8(v, 1);
    };
#pragma unroll
    for (int a = 0; a < 2; ++a)
#pragma unroll
      for (int i = 0; i < 3; ++i) fin(acc[a][i], a, i * 64 + wn * 32, seg0 + i * 64 + wn * 32);
    fin(acce, 0, 192, ecol);
  }
  STAMPE(3);
}

template <int KD> struct Small {
  static constexpr int XB = 16384, WB = 8192, SLOT = XB + WB, NS = 6, NT = KD / 64, NLD = 6, LDS = NS * SLOT;
};
template <int N> DI void small_tail_wait() { wait_vm<N * 6>(); bar(); }

template <int KD, bool BIAS, bool FINAL>
__global__ __launch_bounds__(512, 2) void gemm_small(const u16* __restrict__ af, const u16* __restrict__ wmat, const u16* __restrict__ bias,
                                                     const u16* __restrict__ resid, u16* __restrict__ out, f32x2* __restrict__ st KID_ARG) {
  using C = Small<KD>;
  STAMP0;
  __shared__ __attribute__((aligned(16))) char lds_[C::LDS];
  const lds_ptr lds = (lds_ptr)lds_;
  const unsigned lds0 = (unsigned)(size_t)lds;
  const int lane = threadIdx.x & 63, wid = __builtin_amdgcn_readfirstlane(threadIdx.x >> 6);
  const int xcd = blockIdx.x & 7, idx = blockIdx.x >> 3;
  const int mt = (xcd & 3) * 4 + (idx & 3), nt = (xcd >> 2) * 8 + (idx >> 2);
  if (wid >= 4) {
    const int w = wid - 4;
    const __amdgpu_buffer_rsrc_t rx = mkrsrc(af, S_ * KD * 2), rw = mkrsrc(wmat, KD * D_ * 2);
    const unsigned vx = lane * 16;
    const unsigned vw = (((lane >> 5) * 4 + ((lane >> 2) & 3)) * D_ + ((lane >> 4) & 1) * 32 + (lane & 3) * 8) * 2;
    const unsigned sx0 = (unsigned)(mt * 4 + w) * (KD / 16) * 1024;
    const unsigned sw0 = (unsigned)(w * 16 * D_ + nt * 64) * 2;
    auto issue = [&](int t, int slot) {
      const unsigned lb = lds0 + slot * C::SLOT;
      const unsigned sx = sx0 + t * 4096;
      dma16<0>(rx, vx, sx, lb + w * 4096); dma16<1024>(rx, vx, sx, lb + w * 4096);
      dma16<2048>(rx, vx, sx, lb + w * 4096); dma16<3072>(rx, vx, sx, lb + w * 4096);
      const unsigned kt = (unsigned)t * 64 * D_ * 2;
      dma16<0>(rw, vw, sw0 + kt, lb + C::XB + (2 * w) * 1024);
      dma16<0>(rw, vw, sw0 + kt + 8 * D_ * 2, lb + C::XB + (2 * w + 1) * 1024);
    };
#pragma unroll
    for (int t = 0; t < C::NS; ++t) issue(t, t);
    wait_vm<(C::NS - 1) * C::NLD>(); bar();
    int slot = 0;
    for (int t = 1; t < C::NT - (C::NS - 1); ++t) {
      wait_vm<(C::NS - 2) * C::NLD>(); bar();
      issue(t + C::NS - 1, slot); slot = slot == C::NS - 1 ? 0 : slot + 1;
    }
    small_tail_wait<4>(); small_tail_wait<3>(); small_tail_wait<2>(); small_tail_wait<1>(); small_tail_wait<0>();
    return;
  }
  const int r32 = lane & 31, h = lane >> 5, tt = mt * 4 + wid;
  u32x4 res[4];
  u32x2 bs[8];
#pragma unroll
  for (int s = 0; s < 4; ++s) res[s] = *(const u32x4*)(resid + (((size_t)tt * 64 + nt * 4 + s) * 64 + lane) * 8);
  if (BIAS)
#pragma unroll
    for (int g = 0; g < 8; ++g) bs[g] = *(const u32x2*)(bias + nt * 64 + g * 8 + h * 4);
  f32x16 acc[2];
  acc[0] = f32x16{}; acc[1] = f32x16{};
  const int xo = wid * 4096 + lane * 16;
  const int wo = C::XB + h * 512 + ((lane >> 2) & 3) * 64 + ((lane >> 4) & 1) * 32 + (lane & 3) * 8;
  bf16x8 fx[2][4], fw[2][4][2];
  auto rdw = [&](lds_cptr sl, int i, int s) { return cat8(lds_tr(sl + wo + i * 256 + s * 2048), lds_tr(sl + wo + i * 256 + s * 2048 + 1024)); };
  auto tile = [&](int b, lds_cptr sl, bool RD) {
    const int n = b ^ 1;
#pragma unroll
    for (int s = 0; s < 4; ++s) {
      acc[0] = MFMA32(fw[b][s][0], fx[b][s], acc[0]); SBAR();
      if (RD) { fx[n][s] = lds_frag(sl + xo + s * 1024); fw[n][s][0] = rdw(sl, 0, s); SBAR(); }
      acc[1] = MFMA32(fw[b][s][1], fx[b][s], acc[1]); SBAR();
      if (RD) { fw[n][s][1] = rdw(sl, 1, s); SBAR(); }
    }
  };
  bar();
  STAMPT(1);
#pragma unroll
  for (int s = 0; s < 4; ++s) { fx[0][s] = lds_frag(lds + xo + s * 1024); fw[0][s][0] = rdw(lds, 0, s); fw[0][s][1] = rdw(lds, 1, s); }
  SBAR();
  int slot = 1;
  for (int t = 0; t < C::NT - 2; t += 2) {
    bar_lgkm(); SBAR();
    tile(0, lds + slot * C::SLOT, true);
    slot = slot == C::NS - 1 ? 0 : slot + 1;
    bar_lgkm(); SBAR();
    tile(1, lds + slot * C::SLOT, true);
    slot = slot == C::NS - 1 ? 0 : slot + 1;
  }
  bar_lgkm(); SBAR();
  tile(0, lds + slot * C::SLOT, true);
  tile(1, lds, false);
  STAMPT(2);
  float s1 = 0.f, s2 = 0.f;
#pragma unroll
  for (int s = 0; s < 4; ++s) {
    const f32x16& ac = acc[s >> 1];
    u32x4 o;
#pragma unroll
    for (int i = 0; i < 4; ++i) {
      const int e = (s & 1) * 8 + 2 * i;
      float v0 = ac[e], v1 = ac[e + 1];
      if (BIAS) { const unsigned bb = bs[s * 2 + (i >> 1)][i & 1]; v0 += bflo(bb); v1 += bfhi(bb); }
      v0 += bflo(res[s][i]); v1 += bfhi(res[s][i]);
      o[i] = cvtpk(v0, v1);
      if (!FINAL) { const float a = bflo(o[i]), c = bfhi(o[i]); s1 += a + c; s2 += a * a + c * c; }
    }
    if (FINAL) *(u32x4*)(out + (size_t)(tt * 32 + r32) * D_ + nt * 64 + s * 16 + h * 8) = nat2frag(o);
    else *(u32x4*)(out + (((size_t)tt * 64 + nt * 4 + s) * 64 + lane) * 8) = o;
  }
  if (!FINAL) {
    s1 = swap_add(s1); s2 = swap_add(s2);
    if (h == 0) st[nt * S_ + tt * 32 + r32] = f32x2{s1, s2};
  }
  STAMPE(3);
}

__global__ __launch_bounds__(512, 2) void attn_kernel(const u16* __restrict__ qf, const u16* __restrict__ kvf, u16* __restrict__ ao KID_ARG) {
  __shared__ __attribute__((aligned(16))) float xch[8][17][64];
  STAMP0;
  const int lane = threadIdx.x & 63, wid = __builtin_amdgcn_readfirstlane(threadIdx.x >> 6);
  const int xcd = blockIdx.x & 7, idx = blockIdx.x >> 3;
  const int head = xcd * 4 + (idx >> 3), qb = idx & 7;
  const int qt0 = qb * 8 + (wid & 3) * 2, kh = wid >> 2;
  const bf16x8* qp = (const bf16x8*)(qf + (size_t)(head * 64 + qt0) * 1024) + lane;
  const bf16x8* kv0 = (const bf16x8*)(kvf + (size_t)head * 64 * 2048) + lane;
  bf16x8 q[2][2];
#pragma unroll
  for (int a = 0; a < 2; ++a) { q[a][0] = qp[a * 128]; q[a][1] = qp[a * 128 + 64]; }
  f32x16 negm[2];
  float mref[2];
  {
    const bf16x8 k0 = kv0[0], k1 = kv0[64];
#pragma unroll
    for (int a = 0; a < 2; ++a) {
      f32x16 s = MFMA32(k0, q[a][0], f32x16{});
      s = MFMA32(k1, q[a][1], s);
      float m = s[0];
#pragma unroll
      for (int e = 1; e < 16; ++e) m = __builtin_fmaxf(m, s[e]);
      m = swap_max(m);
      mref[a] = m;
#pragma unroll
      for (int e = 0; e < 16; ++e) negm[a][e] = -m;
    }
  }
  f32x16 o[2];
  o[0] = f32x16{}; o[1] = f32x16{};
  f32x4 ls[2];
  ls[0] = f32x4{}; ls[1] = f32x4{};
  bf16x8 sel;
  { const short one = (lane == 0 || lane == 32 || lane == 17 || lane == 49) ? (short)0x3f80 : (short)0;
    sel = bf16x8{one, one, one, one, one, one, one, one}; }
  const __amdgpu_buffer_rsrc_t rkv = mkrsrc(kvf + (size_t)(head * 64 + kh * 32) * 2048, 32 * 4096);
  const unsigned vl = lane * 16;
  auto ld = [&](int j, int part) { return __builtin_bit_cast(bf16x8, __builtin_amdgcn_raw_buffer_load_b128(rkv, vl + part * 1024, j * 4096, 0)); };
  bf16x8 kb[3][2], vb[3][2];
  kb[0][0] = ld(0, 0); kb[0][1] = ld(0, 1); kb[1][0] = ld(1, 0); kb[1][1] = ld(1, 1);
  vb[0][0] = ld(0, 2); vb[0][1] = ld(0, 3); kb[2][0] = ld(2, 0); kb[2][1] = ld(2, 1); vb[1][0] = ld(1, 2); vb[1][1] = ld(1, 3);
  f32x16 sc[2];
  sc[0] = MFMA32(kb[0][0], q[0][0], negm[0]);
  sc[0] = MFMA32(kb[0][1], q[0][1], sc[0]);
  SBAR();
  STAMPT(1);
  auto unit = [&](int a, const bf16x8* v, const bf16x8* k) {
    const int n = a ^ 1;
    f32x16& s = sc[a];
    sc[n] = MFMA32(k[0], q[n][0], negm[n]); SBAR();
#pragma unroll
    for (int e = 0; e < 4; ++e) s[e] = __builtin_amdgcn_exp2f(s[e]);
    SBAR();
    sc[n] = MFMA32(k[1], q[n][1], sc[n]); SBAR();
#pragma unroll
    for (int e = 4; e < 8; ++e) s[e] = __builtin_amdgcn_exp2f(s[e]);
    const bf16x8 p0 = __builtin_bit_cast(bf16x8, pack8(s, 0));
    SBAR();
#pragma unroll
    for (int e = 8; e < 10; ++e) s[e] = __builtin_amdgcn_exp2f(s[e]);
    SBAR();
    o[a] = MFMA32(v[0], p0, o[a]); SBAR();
#pragma unroll
    for (int e = 10; e < 14; ++e) s[e] = __builtin_amdgcn_exp2f(s[e]);
    SBAR();
    ls[a] = MFMA16(sel, p0, ls[a]); SBAR();
#pragma unroll
    for (int e = 14; e < 16; ++e) s[e] = __builtin_amdgcn_exp2f(s[e]);
    const bf16x8 p1 = __builtin_bit_cast(bf16x8, pack8(s, 1));
    SBAR();
    o[a] = MFMA32(v[1], p1, o[a]); SBAR();
    ls[a] = MFMA16(sel, p1, ls[a]); SBAR();
  };
  auto tile = [&](int j, int c) {
    const int c1 = c == 2 ? 0 : c + 1, c2 = c1 == 2 ? 0 : c1 + 1;
    vb[c2][0] = ld(j + 2, 2); vb[c2][1] = ld(j + 2, 3); SBAR();
    unit(0, vb[c], kb[c]);
    kb[c][0] = ld(j + 3, 0); kb[c][1] = ld(j + 3, 1); SBAR();
    unit(1, vb[c], kb[c1]);
  };
  for (int j = 0; j < 30; j += 3) { tile(j, 0); tile(j + 1, 1); tile(j + 2, 2); }
  tile(30, 0); tile(31, 1);
  STAMPT(2);
  float l[2];
#pragma unroll
  for (int a = 0; a < 2; ++a) {
    const float r0 = __shfl(ls[a][0], lane & 15), r1 = __shfl(ls[a][1], lane & 15);
    l[a] = (lane & 16) ? r1 : r0;
  }
  {
    const f32x16& og = kh ? o[0] : o[1];
#pragma unroll
    for (int e = 0; e < 16; ++e) xch[wid][e][lane] = og[e];
    xch[wid][16][lane] = kh ? l[0] : l[1];
  }
  __syncthreads();
  f32x16 of = kh ? o[1] : o[0];
  float lf = kh ? l[1] : l[0];
#pragma unroll
  for (int e = 0; e < 16; ++e) of[e] += xch[wid ^ 4][e][lane];
  lf += xch[wid ^ 4][16][lane];
  float chk = 0.f;
#pragma unroll
  for (int e = 0; e < 16; ++e) chk = __builtin_fmaxf(chk, __builtin_fabsf(of[e]));
  const bool bad = !(lf < 1.0e18f) || !(chk < 1.0e36f);
  if (__builtin_expect(__any(bad), 0)) {
    const bf16x8 qa = kh ? q[1][0] : q[0][0], qc = kh ? q[1][1] : q[0][1];
    float m = kh ? mref[1] : mref[0];
    of = f32x16{}; lf = 0.f;
    for (int j = 0; j < 64; ++j) {
      f32x16 s = MFMA32(kv0[j * 256], qa, f32x16{});
      s = MFMA32(kv0[j * 256 + 64], qc, s);
      float mx = s[0];
#pragma unroll
      for (int e = 1; e < 16; ++e) mx = __builtin_fmaxf(mx, s[e]);
      mx = __builtin_fmaxf(swap_max(mx), m);
      const float f = __builtin_amdgcn_exp2f(m - mx);
      m = mx;
      float ps = 0.f;
#pragma unroll
      for (int e = 0; e < 16; ++e) { s[e] = __builtin_amdgcn_exp2f(s[e] - m); ps += s[e]; of[e] *= f; }
      lf = lf * f + swap_add(ps);
      of = MFMA32(kv0[j * 256 + 128], __builtin_bit_cast(bf16x8, pack8(s, 0)), of);
      of = MFMA32(kv0[j * 256 + 192], __builtin_bit_cast(bf16x8, pack8(s, 1)), of);
    }
  }
  const float rl = 1.0f / lf;
#pragma unroll
  for (int e = 0; e < 16; ++e) of[e] *= rl;
  u16* dst = ao + (((size_t)(qt0 + kh) * 64 + head * 2) * 64 + lane) * 8;
  *(u32x4*)dst = pack8(of, 0);
  *(u32x4*)(dst + 512) = pack8(of, 1);
  STAMPE(3);
}

}

extern "C" void kernel_launch(void* const* d_in, const int* in_sizes, int n_in, void* d_out, int out_size, void* d_ws, size_t ws_size,
                              hipStream_t stream) {
  const u16* x = (const u16*)d_in[0];
  const u16* wqkv = (const u16*)d_in[1];
  const u16* wo = (const u16*)d_in[2];
  const u16* w1 = (const u16*)d_in[4];
  const u16* b1 = (const u16*)d_in[5];
  const u16* w2 = (const u16*)d_in[6];
  const u16* b2 = (const u16*)d_in[7];
  char* ws = (char*)d_ws;
  u16* X[4] = {(u16*)(ws + WS_X0), (u16*)(ws + WS_X1), (u16*)(ws + WS_X2), (u16*)(ws + WS_X3)};
  f32x2* ST[4] = {(f32x2*)(ws + WS_ST0), (f32x2*)(ws + WS_ST1), (f32x2*)(ws + WS_ST2), (f32x2*)(ws + WS_ST3)};
  u16 *Q = (u16*)(ws + WS_Q), *KV = (u16*)(ws + WS_KV), *AO = (u16*)(ws + WS_AO), *Hb = (u16*)(ws + WS_H);
  float *csq = (float*)(ws + WS_CSQ), *cs1 = (float*)(ws + WS_CS1);
  prep_kernel<<<336, 512, 0, stream>>>(x, wqkv, w1, X[0], ST[0], csq, cs1 KID(0));
  for (int l = 0; l < 2; ++l) {
    gemm_big<0><<<256, 512, 0, stream>>>(X[2 * l], wqkv, ST[2 * l], csq, b1, Q, KV KID(1 + 5 * l));
    attn_kernel<<<256, 512, 0, stream>>>(Q, KV, AO KID(2 + 5 * l));
    gemm_small<1024, false, false><<<256, 512, 0, stream>>>(AO, wo, b2, X[2 * l], X[2 * l + 1], ST[2 * l + 1] KID(3 + 5 * l));
    gemm_big<1><<<256, 512, 0, stream>>>(X[2 * l + 1], w1, ST[2 * l + 1], cs1, b1, Hb, Hb KID(4 + 5 * l));
    if (l == 0) gemm_small<F_, true, false><<<256, 512, 0, stream>>>(Hb, w2, b2, X[1], X[2], ST[2] KID(5));
    else gemm_small<F_, true, true><<<256, 512, 0, stream>>>(Hb, w2, b2, X[3], (u16*)d_out, ST[3] KID(10));
  }
}
```

```cpp
#include <hip/hip_runtime.h>
#include <hip/hip_bf16.h>

#define ST(i)
#define DBG(i)
#define DBG0
#define DBGE
#define STX(i)
#define STK0
#define STK(i)
#define STKE
#define STL(i)
#define STL0(i)
#define KSEL 0
#define ACQ_A 0

#define PF_THR 4
#define PF_SLP 0
#define FAKEW 63
#define OORW 0u
#define OORA 0u
#define HSLP 4
#define TCHS 1
#define FAKEA 63
#define PF_N 8
#define TOUCH 1
#define RAW64 1
#define NTMAP ((xcd >> 1) * 4 + (idx >> 3))
#define MAPQ 0
#define T0P 1
#define ROTA 0
#define TQ 1
#define TKV 0
#define ROT0 2
#define ROT0R 2
#define ROT1 2
#define TCH4 1000
#define TCH4S 4
#define ROT2 0
#define CODEPF 1
#define TCHO 1
#define HSH 0
#define WARMF 2
#define OUTP 0
#define RESW 4
#define OROT 1
#define MAPF 1
#define FMT ((~xcd & 1) * 8 + (xcd & 2) * 2 + (idx & 3))
#define OMT ((~xcd & 1) * 8 + (~xcd & 2) * 2 + (xcd >> 2) * 2 + (idx & 1))
namespace {

typedef short bf16x8 __attribute__((ext_vector_type(8)));
typedef short s16x4 __attribute__((ext_vector_type(4)));
typedef float f32x16 __attribute__((ext_vector_type(16)));
typedef float f32x4 __attribute__((ext_vector_type(4)));
typedef float f32x2 __attribute__((ext_vector_type(2)));
typedef unsigned u32x4 __attribute__((ext_vector_type(4)));
typedef unsigned u32x2 __attribute__((ext_vector_type(2)));
typedef __bf16 bf16x2_t __attribute__((ext_vector_type(2)));
typedef unsigned short u16;
typedef __amdgpu_buffer_rsrc_t rsrc_t;
#define LAS __attribute__((address_space(3)))
typedef LAS const char* lds_cptr;
typedef LAS char* lds_ptr;
#define DI __device__ __forceinline__
#define MFMA32(a, b, c) __builtin_amdgcn_mfma_f32_32x32x16_bf16((a), (b), (c), 0, 0, 0)
#define MFMA16(a, b, c) __builtin_amdgcn_mfma_f32_16x16x32_bf16((a), (b), (c), 0, 0, 0)
typedef float f32x8 __attribute__((ext_vector_type(8)));
typedef __bf16 bfv8 __attribute__((ext_vector_type(8)));
typedef __bf16 bfv16 __attribute__((ext_vector_type(16)));
#define MFMAF(a, b, c) __builtin_amdgcn_smfmac_f32_16x16x64_bf16(__builtin_bit_cast(bfv8, a), __builtin_bit_cast(bfv16, b), (c), sidx, 0, 0)
#define MFMAF32(a, b, c) __builtin_amdgcn_smfmac_f32_32x32x32_bf16(__builtin_bit_cast(bfv8, a), __builtin_bit_cast(bfv16, b), (c), sidx, 0, 0)
#define SBAR() __builtin_amdgcn_sched_barrier(0)

constexpr int S_ = 2048, D_ = 1024, F_ = 3584;
constexpr float EPS = 1e-5f;
constexpr float GA = 0.4686686931f, GB = 4.9122295393f;
constexpr float QSCALE = 0.17677669529663687f * 1.4426950408889634f;

constexpr size_t MB = 1u << 20;
constexpr size_t WS_L = 48 * MB;
constexpr size_t WS_XA = 0, WS_XB = 4 * MB, WS_AO = 8 * MB, WS_KV = 16 * MB, WS_H = 24 * MB, WS_P = 40 * MB;
constexpr size_t WS_STA = 12 * MB, WS_STB = WS_STA + 512 * 1024;
constexpr size_t WS_HF = 38 * MB;
constexpr size_t WS_CSQ = 39 * MB, WS_CS1 = WS_CSQ + 512 * 1024, WS_SG = WS_L + WS_CSQ;
constexpr size_t WS_EP = WS_SG + 32768, WS_FL = 13 * MB;
constexpr size_t WS_W2 = 2 * WS_L;
constexpr int SC1 = 16;

DI unsigned cvtpk(float lo, float hi) { f32x2 v = {lo, hi}; return __builtin_bit_cast(unsigned, __builtin_convertvector(v, bf16x2_t)); }
DI unsigned cvtpk2(f32x2 v) { return __builtin_bit_cast(unsigned, __builtin_convertvector(v, bf16x2_t)); }
DI float bflo(unsigned u) { return __uint_as_float(u << 16); }
DI float bfhi(unsigned u) { return __uint_as_float(u & 0xffff0000u); }
DI f32x2 bf2(unsigned u) { return f32x2{bflo(u), bfhi(u)}; }
DI f32x2 lo2(f32x4 v) { return f32x2{v[0], v[1]}; }
DI f32x2 hi2(f32x4 v) { return f32x2{v[2], v[3]}; }
DI f32x2 fma2(f32x2 a, f32x2 b, f32x2 c) { return __builtin_elementwise_fma(a, b, c); }
DI f32x2 bc2(float a) { return f32x2{a, a}; }
DI float swap_add(float v) {
  auto r = __builtin_amdgcn_permlane32_swap(__float_as_uint(v), __float_as_uint(v), false, false);
  return __uint_as_float(r[0]) + __uint_as_float(r[1]);
}
DI float swap16_add(float v) {
  auto r = __builtin_amdgcn_permlane16_swap(__float_as_uint(v), __float_as_uint(v), false, false);
  return __uint_as_float(r[0]) + __uint_as_float(r[1]);
}
DI float swap_max(float v) {
  auto r = __builtin_amdgcn_permlane32_swap(__float_as_uint(v), __float_as_uint(v), false, false);
  return __builtin_fmaxf(__uint_as_float(r[0]), __uint_as_float(r[1]));
}
DI u32x4 pack8(const f32x16& x, int s) {
  return u32x4{cvtpk(x[8 * s], x[8 * s + 1]), cvtpk(x[8 * s + 2], x[8 * s + 3]), cvtpk(x[8 * s + 4], x[8 * s + 5]), cvtpk(x[8 * s + 6], x[8 * s + 7])};
}
DI u32x4 nat2frag(u32x4 v) {
  auto r0 = __builtin_amdgcn_permlane16_swap(v[0], v[2], false, false);
  auto r1 = __builtin_amdgcn_permlane16_swap(v[1], v[3], false, false);
  return u32x4{r0[0], r1[0], r0[1], r1[1]};
}
DI bf16x8 lds_frag(lds_cptr p) { return *(const LAS bf16x8*)p; }
DI s16x4 lds_tr(lds_cptr p) { return __builtin_bit_cast(s16x4, __builtin_amdgcn_ds_read_tr16_b64_v4i16((LAS s16x4*)p)); }
DI bf16x8 cat8(s16x4 a, s16x4 b) { return __builtin_shufflevector(a, b, 0, 1, 2, 3, 4, 5, 6, 7); }
template <int N> DI void wait_vm() { asm volatile("s_waitcnt vmcnt(%0)" ::"i"(N) : "memory"); }
DI void bar() { asm volatile("s_barrier" ::: "memory"); }
DI void bar_lgkm() { __builtin_amdgcn_s_waitcnt(0xC07F); asm volatile("s_barrier" ::: "memory"); }
template <int IMM> DI void dma16(rsrc_t rs, unsigned voff, unsigned soff, unsigned ldsaddr) {
  asm volatile("s_mov_b32 m0, %0\n\ts_nop 0\n\tbuffer_load_dwordx4 %1, %2, %3 offen offset:%4 lds" ::"s"(ldsaddr), "v"(voff), "s"(rs), "s"(soff), "i"(IMM) : "memory");
}
DI void dma64(rsrc_t rs, unsigned voff, unsigned soff, unsigned ldsaddr) {
  asm volatile("s_mov_b32 m0, %0\n\ts_nop 0\n\tbuffer_load_dwordx4 %1, %2, %3 offen sc1 lds\n\tbuffer_load_dwordx4 %1, %2, %3 offen offset:1024 sc1 lds\n\t"
               "buffer_load_dwordx4 %1, %2, %3 offen offset:2048 sc1 lds\n\tbuffer_load_dwordx4 %1, %2, %3 offen offset:3072 sc1 lds" ::"s"(ldsaddr), "v"(voff), "s"(rs), "s"(soff) : "memory");
}
DI void touch(rsrc_t rs, unsigned voff, unsigned ldsaddr) {
  asm volatile("s_mov_b32 m0, %0\n\ts_nop 0\n\tbuffer_load_dword %1, %2, 0 offen lds" ::"s"(ldsaddr), "v"(voff), "s"(rs) : "memory");
}
template <class T> DI T opq(T v) { asm volatile("" : "+s"(v)); return v; }
DI unsigned opqv(unsigned v) { asm volatile("" : "+v"(v) :: "memory"); return v; }
DI rsrc_t mkrsrc(const void* p, unsigned bytes) { return __builtin_amdgcn_make_buffer_rsrc(opq((void*)p), 0, opq(bytes), opq(0x00020000)); }
DI void st16(u32x4 v, rsrc_t rs, unsigned voff, unsigned soff) { __builtin_amdgcn_raw_buffer_store_b128(v, rs, voff + soff, 0, SC1); }
DI unsigned fblk(int T, int s, int C) { return (unsigned)(((T >> 3) * (C / 64) + (s >> 1)) * 16 + (T & 7) * 2 + (s & 1)) * 1024; }
DI int lane_id() { int v = __builtin_amdgcn_mbcnt_hi(~0u, __builtin_amdgcn_mbcnt_lo(~0u, 0u)); asm volatile("" : "+v"(v)); return v; }
DI int block_id() { return opq((int)blockIdx.x); }
DI void acquire() { asm volatile("buffer_inv sc1" ::: "memory"); }

#define TL 0
#define TWN 16
#define GEMM_HEAD_Q \
  "v_mov_b64 v[0:1], 0\n\tv_mov_b64 v[2:3], 0\n\tv_mov_b64 v[4:5], 0\n\tv_mov_b64 v[6:7], 0\n\tv_mov_b64 v[8:9], 0\n\tv_mov_b64 v[10:11], 0\n\tv_mov_b64 v[12:13], 0\n\t" \
  "v_mov_b64 v[14:15], 0\n\tv_mov_b64 v[16:17], 0\n\tv_mov_b64 v[18:19], 0\n\tv_mov_b64 v[20:21], 0\n\tv_mov_b64 v[22:23], 0\n\tv_mov_b64 v[24:25], 0\n\t" \
  "v_mov_b64 v[26:27], 0\n\tv_mov_b64 v[28:29], 0\n\tv_mov_b64 v[30:31], 0\n\tv_mov_b64 v[32:33], 0\n\tv_mov_b64 v[34:35], 0\n\tv_mov_b64 v[36:37], 0\n\t" \
  "v_mov_b64 v[38:39], 0\n\tv_mov_b64 v[40:41], 0\n\tv_mov_b64 v[42:43], 0\n\tv_mov_b64 v[44:45], 0\n\tv_mov_b64 v[46:47], 0\n\tv_mov_b64 v[48:49], 0\n\t" \
  "v_mov_b64 v[50:51], 0\n\tv_mov_b64 v[52:53], 0\n\tv_mov_b64 v[54:55], 0\n\tv_mov_b64 v[56:57], 0\n\tv_mov_b64 v[58:59], 0\n\tv_mov_b64 v[60:61], 0\n\t" \
  "v_mov_b64 v[62:63], 0\n\tv_mov_b64 v[64:65], 0\n\tv_mov_b64 v[66:67], 0\n\tv_mov_b64 v[68:69], 0\n\tv_mov_b64 v[70:71], 0\n\tv_mov_b64 v[72:73], 0\n\t" \
  "v_mov_b64 v[74:75], 0\n\tv_mov_b64 v[76:77], 0\n\tv_mov_b64 v[78:79], 0\n\tv_mov_b64 v[80:81], 0\n\tv_mov_b64 v[82:83], 0\n\tv_mov_b64 v[84:85], 0\n\t" \
  "v_mov_b64 v[86:87], 0\n\tv_mov_b64 v[88:89], 0\n\tv_mov_b64 v[90:91], 0\n\tv_mov_b64 v[92:93], 0\n\tv_mov_b64 v[94:95], 0\n\ts_mov_b32 %5, %6\n\ts_barrier\n\t"
#define GEMM_LOOP_Q \
  "ds_read_b128 v[112:115], %0 offset:0\n\tds_read_b64_tr_b16 v[128:129], %2 offset:0\n\tds_read_b64_tr_b16 v[130:131], %2 offset:512\n\t" \
  "ds_read_b128 v[116:119], %0 offset:2048\n\tds_read_b128 v[120:123], %1 offset:0\n\tds_read_b128 v[124:127], %1 offset:2048\n\t" \
  "ds_read_b64_tr_b16 v[132:133], %2 offset:32\n\tds_read_b64_tr_b16 v[134:135], %2 offset:544\n\tds_read_b64_tr_b16 v[136:137], %2 offset:8448\n\t" \
  "ds_read_b64_tr_b16 v[138:139], %2 offset:8960\n\tds_read_b64_tr_b16 v[140:141], %2 offset:8480\n\tds_read_b64_tr_b16 v[142:143], %2 offset:8992\n\t" \
  "ds_read_b64_tr_b16 v[144:145], %2 offset:16896\n\tds_read_b64_tr_b16 v[146:147], %2 offset:17408\n\tds_read_b64_tr_b16 v[148:149], %2 offset:16928\n\t" \
  "ds_read_b64_tr_b16 v[150:151], %2 offset:17440\n\ts_cmp_eq_u32 %5, %6\n\ts_waitcnt lgkmcnt(0)\n\ts_branch .Lga%=\n\t.p2align 6\n\t.Lgl%=:\n\t" \
  "v_mfma_f32_16x16x32_bf16 v[0:3], v[176:179], v[160:163], v[0:3]\n\tds_read_b128 v[112:115], %0 offset:0\n\t" \
  "v_mfma_f32_16x16x32_bf16 v[4:7], v[176:179], v[164:167], v[4:7]\n\tds_read_b64_tr_b16 v[128:129], %2 offset:0\n\t" \
  "v_mfma_f32_16x16x32_bf16 v[8:11], v[176:179], v[168:171], v[8:11]\n\tds_read_b64_tr_b16 v[130:131], %2 offset:512\n\t" \
  "v_mfma_f32_16x16x32_bf16 v[12:15], v[176:179], v[172:175], v[12:15]\n\tds_read_b128 v[116:119], %0 offset:2048\n\t" \
  "v_mfma_f32_16x16x32_bf16 v[16:19], v[180:183], v[160:163], v[16:19]\n\tds_read_b128 v[120:123], %1 offset:0\n\t" \
  "v_mfma_f32_16x16x32_bf16 v[20:23], v[180:183], v[164:167], v[20:23]\n\tds_read_b128 v[124:127], %1 offset:2048\n\t" \
  "v_mfma_f32_16x16x32_bf16 v[24:27], v[180:183], v[168:171], v[24:27]\n\tds_read_b64_tr_b16 v[132:133], %2 offset:32\n\t" \
  "v_mfma_f32_16x16x32_bf16 v[28:31], v[180:183], v[172:175], v[28:31]\n\tds_read_b64_tr_b16 v[134:135], %2 offset:544\n\t" \
  "v_mfma_f32_16x16x32_bf16 v[32:35], v[184:187], v[160:163], v[32:35]\n\tds_read_b64_tr_b16 v[136:137], %2 offset:8448\n\t" \
  "v_mfma_f32_16x16x32_bf16 v[36:39], v[184:187], v[164:167], v[36:39]\n\tds_read_b64_tr_b16 v[138:139], %2 offset:8960\n\t" \
  "v_mfma_f32_16x16x32_bf16 v[40:43], v[184:187], v[168:171], v[40:43]\n\tds_read_b64_tr_b16 v[140:141], %2 offset:8480\n\t" \
  "v_mfma_f32_16x16x32_bf16 v[44:47], v[184:187], v[172:175], v[44:47]\n\tds_read_b64_tr_b16 v[142:143], %2 offset:8992\n\t" \
  "v_mfma_f32_16x16x32_bf16 v[48:51], v[188:191], v[160:163], v[48:51]\n\tds_read_b64_tr_b16 v[144:145], %2 offset:16896\n\t" \
  "v_mfma_f32_16x16x32_bf16 v[52:55], v[188:191], v[164:167], v[52:55]\n\tds_read_b64_tr_b16 v[146:147], %2 offset:17408\n\t" \
  "v_mfma_f32_16x16x32_bf16 v[56:59], v[188:191], v[168:171], v[56:59]\n\tds_read_b64_tr_b16 v[148:149], %2 offset:16928\n\t" \
  "v_mfma_f32_16x16x32_bf16 v[60:63], v[188:191], v[172:175], v[60:63]\n\tds_read_b64_tr_b16 v[150:151], %2 offset:17440\n\t" \
  "v_mfma_f32_16x16x32_bf16 v[64:67], v[160:163], v[192:195], v[64:67]\n\ts_bitcmp1_b32 %4, 2\n\tv_mfma_f32_16x16x32_bf16 v[68:71], v[164:167], v[192:195], v[68:71]\n\t" \
  "s_cselect_b32 %5, %7, %6\n\tv_mfma_f32_16x16x32_bf16 v[72:75], v[168:171], v[192:195], v[72:75]\n\ts_lshr_b32 %4, %4, 2\n\t" \
  "v_mfma_f32_16x16x32_bf16 v[76:79], v[172:175], v[192:195], v[76:79]\n\tv_mfma_f32_16x16x32_bf16 v[80:83], v[160:163], v[196:199], v[80:83]\n\t" \
  "v_mfma_f32_16x16x32_bf16 v[84:87], v[164:167], v[196:199], v[84:87]\n\tv_mfma_f32_16x16x32_bf16 v[88:91], v[168:171], v[196:199], v[88:91]\n\ts_waitcnt lgkmcnt(0)\n\t" \
  "v_mfma_f32_16x16x32_bf16 v[92:95], v[172:175], v[196:199], v[92:95]\n\t.Lga%=:\n\tv_mfma_f32_16x16x32_bf16 v[0:3], v[128:131], v[112:115], v[0:3]\n\t" \
  "ds_read_b128 v[160:163], %0 offset:1024\n\tv_mfma_f32_16x16x32_bf16 v[4:7], v[128:131], v[116:119], v[4:7]\n\tds_read_b64_tr_b16 v[176:177], %2 offset:4224\n\t" \
  "v_mfma_f32_16x16x32_bf16 v[8:11], v[128:131], v[120:123], v[8:11]\n\tds_read_b64_tr_b16 v[178:179], %2 offset:4736\n\t" \
  "v_mfma_f32_16x16x32_bf16 v[12:15], v[128:131], v[124:127], v[12:15]\n\tds_read_b128 v[164:167], %0 offset:3072\n\t" \
  "v_mfma_f32_16x16x32_bf16 v[16:19], v[132:135], v[112:115], v[16:19]\n\tds_read_b128 v[168:171], %1 offset:1024\n\t" \
  "v_mfma_f32_16x16x32_bf16 v[20:23], v[132:135], v[116:119], v[20:23]\n\tds_read_b128 v[172:175], %1 offset:3072\n\t" \
  "v_mfma_f32_16x16x32_bf16 v[24:27], v[132:135], v[120:123], v[24:27]\n\tds_read_b64_tr_b16 v[180:181], %2 offset:4256\n\t" \
  "v_mfma_f32_16x16x32_bf16 v[28:31], v[132:135], v[124:127], v[28:31]\n\tds_read_b64_tr_b16 v[182:183], %2 offset:4768\n\t" \
  "v_mfma_f32_16x16x32_bf16 v[32:35], v[136:139], v[112:115], v[32:35]\n\tds_read_b64_tr_b16 v[184:185], %2 offset:12672\n\t" \
  "v_mfma_f32_16x16x32_bf16 v[36:39], v[136:139], v[116:119], v[36:39]\n\tds_read_b64_tr_b16 v[186:187], %2 offset:13184\n\t" \
  "v_mfma_f32_16x16x32_bf16 v[40:43], v[136:139], v[120:123], v[40:43]\n\tds_read_b64_tr_b16 v[188:189], %2 offset:12704\n\t" \
  "v_mfma_f32_16x16x32_bf16 v[44:47], v[136:139], v[124:127], v[44:47]\n\tds_read_b64_tr_b16 v[190:191], %2 offset:13216\n\t" \
  "v_mfma_f32_16x16x32_bf16 v[48:51], v[140:143], v[112:115], v[48:51]\n\tds_read_b64_tr_b16 v[192:193], %2 offset:21120\n\t" \
  "v_mfma_f32_16x16x32_bf16 v[52:55], v[140:143], v[116:119], v[52:55]\n\tds_read_b64_tr_b16 v[194:195], %2 offset:21632\n\t" \
  "v_mfma_f32_16x16x32_bf16 v[56:59], v[140:143], v[120:123], v[56:59]\n\tds_read_b64_tr_b16 v[196:197], %2 offset:21152\n\t" \
  "v_mfma_f32_16x16x32_bf16 v[60:63], v[140:143], v[124:127], v[60:63]\n\tds_read_b64_tr_b16 v[198:199], %2 offset:21664\n\t" \
  "v_mfma_f32_16x16x32_bf16 v[64:67], v[112:115], v[144:147], v[64:67]\n\tv_add_u32 %0, %5, %0\n\tv_mfma_f32_16x16x32_bf16 v[68:71], v[116:119], v[144:147], v[68:71]\n\t" \
  "v_add_u32 %1, %5, %1\n\tv_mfma_f32_16x16x32_bf16 v[72:75], v[120:123], v[144:147], v[72:75]\n\tv_add_u32 %2, %5, %2\n\t" \
  "v_mfma_f32_16x16x32_bf16 v[76:79], v[124:127], v[144:147], v[76:79]\n\tv_mfma_f32_16x16x32_bf16 v[80:83], v[112:115], v[148:151], v[80:83]\n\t" \
  "v_mfma_f32_16x16x32_bf16 v[84:87], v[116:119], v[148:151], v[84:87]\n\ts_waitcnt lgkmcnt(0)\n\tv_mfma_f32_16x16x32_bf16 v[88:91], v[120:123], v[148:151], v[88:91]\n\t" \
  "s_barrier\n\tv_mfma_f32_16x16x32_bf16 v[92:95], v[124:127], v[148:151], v[92:95]\n\ts_cbranch_scc1 .Lgl%=\n\t" \
  "v_mfma_f32_16x16x32_bf16 v[0:3], v[176:179], v[160:163], v[0:3]\n\tds_read_b32 v144, v224 offset:0\n\tv_mfma_f32_16x16x32_bf16 v[4:7], v[176:179], v[164:167], v[4:7]\n\t" \
  "ds_read_b32 v152, v224 offset:512\n\tv_mfma_f32_16x16x32_bf16 v[8:11], v[176:179], v[168:171], v[8:11]\n\tds_read_b32 v146, v224 offset:64\n\t" \
  "v_mfma_f32_16x16x32_bf16 v[12:15], v[176:179], v[172:175], v[12:15]\n\tds_read_b32 v154, v224 offset:576\n\t" \
  "v_mfma_f32_16x16x32_bf16 v[16:19], v[180:183], v[160:163], v[16:19]\n\tds_read_b32 v148, v225 offset:0\n\t" \
  "v_mfma_f32_16x16x32_bf16 v[20:23], v[180:183], v[164:167], v[20:23]\n\tds_read_b32 v156, v225 offset:512\n\t" \
  "v_mfma_f32_16x16x32_bf16 v[24:27], v[180:183], v[168:171], v[24:27]\n\tds_read_b32 v150, v225 offset:64\n\t" \
  "v_mfma_f32_16x16x32_bf16 v[28:31], v[180:183], v[172:175], v[28:31]\n\tds_read_b32 v158, v225 offset:576\n\t" \
  "v_mfma_f32_16x16x32_bf16 v[32:35], v[184:187], v[160:163], v[32:35]\n\tds_read_b128 v[120:123], v226 offset:256\n\t" \
  "v_mfma_f32_16x16x32_bf16 v[36:39], v[184:187], v[164:167], v[36:39]\n\tds_read_b128 v[124:127], v226 offset:320\n\t" \
  "v_mfma_f32_16x16x32_bf16 v[40:43], v[184:187], v[168:171], v[40:43]\n\tds_read_b128 v[112:115], v226 offset:0\n\t" \
  "v_mfma_f32_16x16x32_bf16 v[44:47], v[184:187], v[172:175], v[44:47]\n\tds_read_b128 v[116:119], v226 offset:64\n\t" \
  "v_mfma_f32_16x16x32_bf16 v[48:51], v[188:191], v[160:163], v[48:51]\n\tds_read_b128 v[128:131], v227 offset:0\n\t" \
  "v_mfma_f32_16x16x32_bf16 v[52:55], v[188:191], v[164:167], v[52:55]\n\tds_read_b128 v[132:135], v227 offset:64\n\t" \
  "v_mfma_f32_16x16x32_bf16 v[56:59], v[188:191], v[168:171], v[56:59]\n\tds_read_b128 v[136:139], v227 offset:512\n\t" \
  "v_mfma_f32_16x16x32_bf16 v[60:63], v[188:191], v[172:175], v[60:63]\n\tds_read_b128 v[140:143], v227 offset:576\n\t" \
  "v_mfma_f32_16x16x32_bf16 v[64:67], v[160:163], v[192:195], v[64:67]\n\tv_mfma_f32_16x16x32_bf16 v[68:71], v[164:167], v[192:195], v[68:71]\n\t" \
  "v_mfma_f32_16x16x32_bf16 v[72:75], v[168:171], v[192:195], v[72:75]\n\tv_mfma_f32_16x16x32_bf16 v[76:79], v[172:175], v[192:195], v[76:79]\n\t" \
  "v_mfma_f32_16x16x32_bf16 v[80:83], v[160:163], v[196:199], v[80:83]\n\tv_mfma_f32_16x16x32_bf16 v[84:87], v[164:167], v[196:199], v[84:87]\n\t" \
  "v_mfma_f32_16x16x32_bf16 v[88:91], v[168:171], v[196:199], v[88:91]\n\ts_waitcnt lgkmcnt(0)\n\tv_mfma_f32_16x16x32_bf16 v[92:95], v[172:175], v[196:199], v[92:95]\n\t" \
  "s_nop 7\n\ts_nop 7\n\t"
#define GEMM_HEAD_F \
  "v_mov_b64 v[0:1], 0\n\tv_mov_b64 v[2:3], 0\n\tv_mov_b64 v[4:5], 0\n\tv_mov_b64 v[6:7], 0\n\tv_mov_b64 v[8:9], 0\n\tv_mov_b64 v[10:11], 0\n\tv_mov_b64 v[12:13], 0\n\t" \
  "v_mov_b64 v[14:15], 0\n\tv_mov_b64 v[16:17], 0\n\tv_mov_b64 v[18:19], 0\n\tv_mov_b64 v[20:21], 0\n\tv_mov_b64 v[22:23], 0\n\tv_mov_b64 v[24:25], 0\n\t" \
  "v_mov_b64 v[26:27], 0\n\tv_mov_b64 v[28:29], 0\n\tv_mov_b64 v[30:31], 0\n\tv_mov_b64 v[32:33], 0\n\tv_mov_b64 v[34:35], 0\n\tv_mov_b64 v[36:37], 0\n\t" \
  "v_mov_b64 v[38:39], 0\n\tv_mov_b64 v[40:41], 0\n\tv_mov_b64 v[42:43], 0\n\tv_mov_b64 v[44:45], 0\n\tv_mov_b64 v[46:47], 0\n\tv_mov_b64 v[48:49], 0\n\t" \
  "v_mov_b64 v[50:51], 0\n\tv_mov_b64 v[52:53], 0\n\tv_mov_b64 v[54:55], 0\n\tv_mov_b64 v[56:57], 0\n\tv_mov_b64 v[58:59], 0\n\tv_mov_b64 v[60:61], 0\n\t" \
  "v_mov_b64 v[62:63], 0\n\tv_mov_b64 v[64:65], 0\n\tv_mov_b64 v[66:67], 0\n\tv_mov_b64 v[68:69], 0\n\tv_mov_b64 v[70:71], 0\n\tv_mov_b64 v[72:73], 0\n\t" \
  "v_mov_b64 v[74:75], 0\n\tv_mov_b64 v[76:77], 0\n\tv_mov_b64 v[78:79], 0\n\tv_mov_b64 v[80:81], 0\n\tv_mov_b64 v[82:83], 0\n\tv_mov_b64 v[84:85], 0\n\t" \
  "v_mov_b64 v[86:87], 0\n\tv_mov_b64 v[88:89], 0\n\tv_mov_b64 v[90:91], 0\n\tv_mov_b64 v[92:93], 0\n\tv_mov_b64 v[94:95], 0\n\tv_mov_b64 v[96:97], 0\n\t" \
  "v_mov_b64 v[98:99], 0\n\tv_mov_b64 v[100:101], 0\n\tv_mov_b64 v[102:103], 0\n\tv_mov_b64 v[104:105], 0\n\tv_mov_b64 v[106:107], 0\n\tv_mov_b64 v[108:109], 0\n\t" \
  "v_mov_b64 v[110:111], 0\n\ts_mov_b32 %5, %6\n\ts_barrier\n\t"
#define GEMM_LOOP_F \
  "ds_read_b128 v[112:115], %0 offset:0\n\tds_read_b64_tr_b16 v[128:129], %2 offset:0\n\tds_read_b64_tr_b16 v[130:131], %2 offset:512\n\t" \
  "ds_read_b128 v[116:119], %0 offset:2048\n\tds_read_b128 v[120:123], %1 offset:0\n\tds_read_b128 v[124:127], %1 offset:2048\n\t" \
  "ds_read_b64_tr_b16 v[132:133], %2 offset:32\n\tds_read_b64_tr_b16 v[134:135], %2 offset:544\n\tds_read_b64_tr_b16 v[136:137], %2 offset:8448\n\t" \
  "ds_read_b64_tr_b16 v[138:139], %2 offset:8960\n\tds_read_b64_tr_b16 v[140:141], %2 offset:8480\n\tds_read_b64_tr_b16 v[142:143], %2 offset:8992\n\t" \
  "ds_read_b64_tr_b16 v[144:145], %2 offset:16896\n\tds_read_b64_tr_b16 v[146:147], %2 offset:17408\n\tds_read_b64_tr_b16 v[148:149], %2 offset:16928\n\t" \
  "ds_read_b64_tr_b16 v[150:151], %2 offset:17440\n\tds_read_b64_tr_b16 v[152:153], %3 offset:0\n\tds_read_b64_tr_b16 v[154:155], %3 offset:256\n\t" \
  "ds_read_b64_tr_b16 v[156:157], %3 offset:32\n\tds_read_b64_tr_b16 v[158:159], %3 offset:288\n\tbuffer_load_dwordx4 v[208:211], v227, %[rr], 0 offen offset:0 sc1\n\t" \
  "buffer_load_dwordx4 v[212:215], v227, %[rr], 0 offen offset:2048 sc1\n\tbuffer_load_dwordx4 v[216:219], v227, %[rr], %[r4] offen offset:0 sc1\n\t" \
  "buffer_load_dwordx4 v[220:223], v227, %[rr], %[r4] offen offset:2048 sc1\n\ts_cmp_eq_u32 %5, %6\n\ts_waitcnt lgkmcnt(0)\n\ts_branch .Lga%=\n\t.p2align 6\n\t.Lgl%=:\n\t" \
  "v_mfma_f32_16x16x32_bf16 v[0:3], v[176:179], v[160:163], v[0:3]\n\tds_read_b128 v[112:115], %0 offset:0\n\t" \
  "v_mfma_f32_16x16x32_bf16 v[4:7], v[176:179], v[164:167], v[4:7]\n\tds_read_b64_tr_b16 v[128:129], %2 offset:0\n\t" \
  "v_mfma_f32_16x16x32_bf16 v[8:11], v[176:179], v[168:171], v[8:11]\n\tds_read_b64_tr_b16 v[130:131], %2 offset:512\n\t" \
  "v_mfma_f32_16x16x32_bf16 v[12:15], v[176:179], v[172:175], v[12:15]\n\tds_read_b128 v[116:119], %0 offset:2048\n\t" \
  "v_mfma_f32_16x16x32_bf16 v[16:19], v[180:183], v[160:163], v[16:19]\n\tds_read_b128 v[120:123], %1 offset:0\n\t" \
  "v_mfma_f32_16x16x32_bf16 v[20:23], v[180:183], v[164:167], v[20:23]\n\tds_read_b128 v[124:127], %1 offset:2048\n\t" \
  "v_mfma_f32_16x16x32_bf16 v[24:27], v[180:183], v[168:171], v[24:27]\n\tds_read_b64_tr_b16 v[132:133], %2 offset:32\n\t" \
  "v_mfma_f32_16x16x32_bf16 v[28:31], v[180:183], v[172:175], v[28:31]\n\tds_read_b64_tr_b16 v[134:135], %2 offset:544\n\t" \
  "v_mfma_f32_16x16x32_bf16 v[32:35], v[184:187], v[160:163], v[32:35]\n\tds_read_b64_tr_b16 v[136:137], %2 offset:8448\n\t" \
  "v_mfma_f32_16x16x32_bf16 v[36:39], v[184:187], v[164:167], v[36:39]\n\tds_read_b64_tr_b16 v[138:139], %2 offset:8960\n\t" \
  "v_mfma_f32_16x16x32_bf16 v[40:43], v[184:187], v[168:171], v[40:43]\n\tds_read_b64_tr_b16 v[140:141], %2 offset:8480\n\t" \
  "v_mfma_f32_16x16x32_bf16 v[44:47], v[184:187], v[172:175], v[44:47]\n\tds_read_b64_tr_b16 v[142:143], %2 offset:8992\n\t" \
  "v_mfma_f32_16x16x32_bf16 v[48:51], v[188:191], v[160:163], v[48:51]\n\tds_read_b64_tr_b16 v[144:145], %2 offset:16896\n\t" \
  "v_mfma_f32_16x16x32_bf16 v[52:55], v[188:191], v[164:167], v[52:55]\n\tds_read_b64_tr_b16 v[146:147], %2 offset:17408\n\t" \
  "v_mfma_f32_16x16x32_bf16 v[56:59], v[188:191], v[168:171], v[56:59]\n\tds_read_b64_tr_b16 v[148:149], %2 offset:16928\n\t" \
  "v_mfma_f32_16x16x32_bf16 v[60:63], v[188:191], v[172:175], v[60:63]\n\tds_read_b64_tr_b16 v[150:151], %2 offset:17440\n\t" \
  "v_mfma_f32_16x16x32_bf16 v[64:67], v[192:195], v[160:163], v[64:67]\n\tds_read_b64_tr_b16 v[152:153], %3 offset:0\n\t" \
  "v_mfma_f32_16x16x32_bf16 v[68:71], v[192:195], v[164:167], v[68:71]\n\tds_read_b64_tr_b16 v[154:155], %3 offset:256\n\t" \
  "v_mfma_f32_16x16x32_bf16 v[72:75], v[192:195], v[168:171], v[72:75]\n\tds_read_b64_tr_b16 v[156:157], %3 offset:32\n\t" \
  "v_mfma_f32_16x16x32_bf16 v[76:79], v[192:195], v[172:175], v[76:79]\n\tds_read_b64_tr_b16 v[158:159], %3 offset:288\n\t" \
  "v_mfma_f32_16x16x32_bf16 v[80:83], v[196:199], v[160:163], v[80:83]\n\ts_bitcmp1_b32 %4, 2\n\tv_mfma_f32_16x16x32_bf16 v[84:87], v[196:199], v[164:167], v[84:87]\n\t" \
  "s_cselect_b32 %5, %7, %6\n\tv_mfma_f32_16x16x32_bf16 v[88:91], v[196:199], v[168:171], v[88:91]\n\ts_lshr_b32 %4, %4, 2\n\t" \
  "v_mfma_f32_16x16x32_bf16 v[92:95], v[196:199], v[172:175], v[92:95]\n\tv_mfma_f32_16x16x32_bf16 v[96:99], v[200:203], v[160:163], v[96:99]\n\t" \
  "v_mfma_f32_16x16x32_bf16 v[100:103], v[200:203], v[164:167], v[100:103]\n\tv_mfma_f32_16x16x32_bf16 v[104:107], v[204:207], v[160:163], v[104:107]\n\t" \
  "s_waitcnt lgkmcnt(0)\n\tv_mfma_f32_16x16x32_bf16 v[108:111], v[204:207], v[164:167], v[108:111]\n\t.Lga%=:\n\t" \
  "v_mfma_f32_16x16x32_bf16 v[0:3], v[128:131], v[112:115], v[0:3]\n\tds_read_b128 v[160:163], %0 offset:1024\n\t" \
  "v_mfma_f32_16x16x32_bf16 v[4:7], v[128:131], v[116:119], v[4:7]\n\tds_read_b64_tr_b16 v[176:177], %2 offset:4224\n\t" \
  "v_mfma_f32_16x16x32_bf16 v[8:11], v[128:131], v[120:123], v[8:11]\n\tds_read_b64_tr_b16 v[178:179], %2 offset:4736\n\t" \
  "v_mfma_f32_16x16x32_bf16 v[12:15], v[128:131], v[124:127], v[12:15]\n\tds_read_b128 v[164:167], %0 offset:3072\n\t" \
  "v_mfma_f32_16x16x32_bf16 v[16:19], v[132:135], v[112:115], v[16:19]\n\tds_read_b128 v[168:171], %1 offset:1024\n\t" \
  "v_mfma_f32_16x16x32_bf16 v[20:23], v[132:135], v[116:119], v[20:23]\n\tds_read_b128 v[172:175], %1 offset:3072\n\t" \
  "v_mfma_f32_16x16x32_bf16 v[24:27], v[132:135], v[120:123], v[24:27]\n\tds_read_b64_tr_b16 v[180:181], %2 offset:4256\n\t" \
  "v_mfma_f32_16x16x32_bf16 v[28:31], v[132:135], v[124:127], v[28:31]\n\tds_read_b64_tr_b16 v[182:183], %2 offset:4768\n\t" \
  "v_mfma_f32_16x16x32_bf16 v[32:35], v[136:139], v[112:115], v[32:35]\n\tds_read_b64_tr_b16 v[184:185], %2 offset:12672\n\t" \
  "v_mfma_f32_16x16x32_bf16 v[36:39], v[136:139], v[116:119], v[36:39]\n\tds_read_b64_tr_b16 v[186:187], %2 offset:13184\n\t" \
  "v_mfma_f32_16x16x32_bf16 v[40:43], v[136:139], v[120:123], v[40:43]\n\tds_read_b64_tr_b16 v[188:189], %2 offset:12704\n\t" \
  "v_mfma_f32_16x16x32_bf16 v[44:47], v[136:139], v[124:127], v[44:47]\n\tds_read_b64_tr_b16 v[190:191], %2 offset:13216\n\t" \
  "v_mfma_f32_16x16x32_bf16 v[48:51], v[140:143], v[112:115], v[48:51]\n\tds_read_b64_tr_b16 v[192:193], %2 offset:21120\n\t" \
  "v_mfma_f32_16x16x32_bf16 v[52:55], v[140:143], v[116:119], v[52:55]\n\tds_read_b64_tr_b16 v[194:195], %2 offset:21632\n\t" \
  "v_mfma_f32_16x16x32_bf16 v[56:59], v[140:143], v[120:123], v[56:59]\n\tds_read_b64_tr_b16 v[196:197], %2 offset:21152\n\t" \
  "v_mfma_f32_16x16x32_bf16 v[60:63], v[140:143], v[124:127], v[60:63]\n\tds_read_b64_tr_b16 v[198:199], %2 offset:21664\n\t" \
  "v_mfma_f32_16x16x32_bf16 v[64:67], v[144:147], v[112:115], v[64:67]\n\tds_read_b64_tr_b16 v[200:201], %3 offset:512\n\t" \
  "v_mfma_f32_16x16x32_bf16 v[68:71], v[144:147], v[116:119], v[68:71]\n\tds_read_b64_tr_b16 v[202:203], %3 offset:768\n\t" \
  "v_mfma_f32_16x16x32_bf16 v[72:75], v[144:147], v[120:123], v[72:75]\n\tds_read_b64_tr_b16 v[204:205], %3 offset:544\n\t" \
  "v_mfma_f32_16x16x32_bf16 v[76:79], v[144:147], v[124:127], v[76:79]\n\tds_read_b64_tr_b16 v[206:207], %3 offset:800\n\t" \
  "v_mfma_f32_16x16x32_bf16 v[80:83], v[148:151], v[112:115], v[80:83]\n\tv_add_u32 %0, %5, %0\n\tv_mfma_f32_16x16x32_bf16 v[84:87], v[148:151], v[116:119], v[84:87]\n\t" \
  "v_add_u32 %1, %5, %1\n\tv_mfma_f32_16x16x32_bf16 v[88:91], v[148:151], v[120:123], v[88:91]\n\tv_add_u32 %2, %5, %2\n\t" \
  "v_mfma_f32_16x16x32_bf16 v[92:95], v[148:151], v[124:127], v[92:95]\n\tv_add_u32 %3, %5, %3\n\tv_mfma_f32_16x16x32_bf16 v[96:99], v[152:155], v[112:115], v[96:99]\n\t" \
  "v_mfma_f32_16x16x32_bf16 v[100:103], v[152:155], v[116:119], v[100:103]\n\ts_waitcnt lgkmcnt(0)\n\t" \
  "v_mfma_f32_16x16x32_bf16 v[104:107], v[156:159], v[112:115], v[104:107]\n\ts_barrier\n\tv_mfma_f32_16x16x32_bf16 v[108:111], v[156:159], v[116:119], v[108:111]\n\t" \
  "s_cbranch_scc1 .Lgl%=\n\tv_mfma_f32_16x16x32_bf16 v[0:3], v[176:179], v[160:163], v[0:3]\n\tds_read_b32 v144, v224 offset:0\n\t" \
  "v_mfma_f32_16x16x32_bf16 v[4:7], v[176:179], v[164:167], v[4:7]\n\tds_read_b32 v152, v224 offset:512\n\t" \
  "v_mfma_f32_16x16x32_bf16 v[8:11], v[176:179], v[168:171], v[8:11]\n\tds_read_b32 v146, v224 offset:64\n\t" \
  "v_mfma_f32_16x16x32_bf16 v[12:15], v[176:179], v[172:175], v[12:15]\n\tds_read_b32 v154, v224 offset:576\n\t" \
  "v_mfma_f32_16x16x32_bf16 v[16:19], v[180:183], v[160:163], v[16:19]\n\tds_read_b32 v148, v225 offset:0\n\t" \
  "v_mfma_f32_16x16x32_bf16 v[20:23], v[180:183], v[164:167], v[20:23]\n\tds_read_b32 v156, v225 offset:512\n\t" \
  "v_mfma_f32_16x16x32_bf16 v[24:27], v[180:183], v[168:171], v[24:27]\n\tds_read_b32 v150, v225 offset:64\n\t" \
  "v_mfma_f32_16x16x32_bf16 v[28:31], v[180:183], v[172:175], v[28:31]\n\tds_read_b32 v158, v225 offset:576\n\t" \
  "v_mfma_f32_16x16x32_bf16 v[32:35], v[184:187], v[160:163], v[32:35]\n\tds_read_b128 v[112:115], v226 offset:2048\n\t" \
  "v_mfma_f32_16x16x32_bf16 v[36:39], v[184:187], v[164:167], v[36:39]\n\tds_read_b128 v[116:119], v226 offset:2112\n\t" \
  "v_mfma_f32_16x16x32_bf16 v[40:43], v[184:187], v[168:171], v[40:43]\n\tds_read_b128 v[120:123], v226 offset:0\n\t" \
  "v_mfma_f32_16x16x32_bf16 v[44:47], v[184:187], v[172:175], v[44:47]\n\tds_read_b128 v[124:127], v226 offset:64\n\t" \
  "v_mfma_f32_16x16x32_bf16 v[48:51], v[188:191], v[160:163], v[48:51]\n\tds_read_b128 v[128:131], v226 offset:2304\n\t" \
  "v_mfma_f32_16x16x32_bf16 v[52:55], v[188:191], v[164:167], v[52:55]\n\tds_read_b128 v[132:135], v226 offset:2368\n\t" \
  "v_mfma_f32_16x16x32_bf16 v[56:59], v[188:191], v[168:171], v[56:59]\n\tds_read_b128 v[136:139], v226 offset:256\n\t" \
  "v_mfma_f32_16x16x32_bf16 v[60:63], v[188:191], v[172:175], v[60:63]\n\tds_read_b128 v[140:143], v226 offset:320\n\t" \
  "v_mfma_f32_16x16x32_bf16 v[64:67], v[192:195], v[160:163], v[64:67]\n\tv_mfma_f32_16x16x32_bf16 v[68:71], v[192:195], v[164:167], v[68:71]\n\t" \
  "v_mfma_f32_16x16x32_bf16 v[72:75], v[192:195], v[168:171], v[72:75]\n\tv_mfma_f32_16x16x32_bf16 v[76:79], v[192:195], v[172:175], v[76:79]\n\t" \
  "v_mfma_f32_16x16x32_bf16 v[80:83], v[196:199], v[160:163], v[80:83]\n\tv_mfma_f32_16x16x32_bf16 v[84:87], v[196:199], v[164:167], v[84:87]\n\t" \
  "v_mfma_f32_16x16x32_bf16 v[88:91], v[196:199], v[168:171], v[88:91]\n\tv_mfma_f32_16x16x32_bf16 v[92:95], v[196:199], v[172:175], v[92:95]\n\t" \
  "v_mfma_f32_16x16x32_bf16 v[96:99], v[200:203], v[160:163], v[96:99]\n\tv_mfma_f32_16x16x32_bf16 v[100:103], v[200:203], v[164:167], v[100:103]\n\t" \
  "v_mfma_f32_16x16x32_bf16 v[104:107], v[204:207], v[160:163], v[104:107]\n\ts_waitcnt vmcnt(0) lgkmcnt(0)\n\t" \
  "v_mfma_f32_16x16x32_bf16 v[108:111], v[204:207], v[164:167], v[108:111]\n\ts_nop 7\n\ts_nop 7\n\t"
#define GEMM_CLOB \
  "v160", "v161", "v162", "v163", "v164", "v165", "v166", "v167", "v168", "v169", "v170", "v171", "v172", "v173", "v174", "v175", "v176", "v177", "v178", "v179", "v180", \
  "v181", "v182", "v183", "v184", "v185", "v186", "v187", "v188", "v189", "v190", "v191", "v192", "v193", "v194", "v195", "v196", "v197", "v198", "v199", "v200", "v201", \
  "v202", "v203", "v204", "v205", "v206", "v207"
#define LOAD_R0 \
  "s_nop 4\n\ts_add_u32 m0, %[mx], 0\n\ts_nop 0\n\tbuffer_load_dwordx4 v48, %[rx], %0 offen lds\n\ts_add_u32 m0, %[mx], 1024\n\ts_nop 0\n\t" \
  "buffer_load_dwordx4 v49, %[rx], %0 offen lds\n\ts_add_u32 m0, %[mx], 2048\n\ts_nop 0\n\tbuffer_load_dwordx4 v50, %[rx], %0 offen lds\n\ts_add_u32 m0, %[mx], 3072\n\ts_nop 0\n\t" \
  "buffer_load_dwordx4 v51, %[rx], %0 offen lds\n\ts_add_u32 %0, %0, 128\n\ts_bitset0_b32 %0, 11\n\ts_add_u32 m0, %[mb], 0\n\ts_nop 0\n\t" \
  "buffer_load_dwordx4 v52, %[rw], %1 offen lds\n\ts_add_u32 m0, %[mb], 4224\n\ts_nop 0\n\tbuffer_load_dwordx4 v53, %[rw], %1 offen lds\n\ts_add_u32 m0, %[mb], 8448\n\ts_nop 0\n\t" \
  "buffer_load_dwordx4 v54, %[rw], %1 offen lds\n\ts_add_u32 m0, %[mb], 12672\n\ts_nop 0\n\tbuffer_load_dwordx4 v55, %[rw], %1 offen lds\n\ts_add_u32 m0, %[mb], 16896\n\ts_nop 0\n\t" \
  "buffer_load_dwordx4 v56, %[rw], %1 offen lds\n\ts_add_u32 m0, %[mb], 21120\n\ts_nop 0\n\tbuffer_load_dwordx4 v57, %[rw], %1 offen lds\n\ts_add_u32 %1, %1, 393216\n\t" \
  "s_sub_u32 vcc_lo, %1, 6291456\n\ts_min_u32 %1, %1, vcc_lo\n\ts_add_u32 m0, %[mx], 41728\n\ts_nop 0\n\tbuffer_load_dwordx4 v48, %[rx], %0 offen lds\n\t" \
  "s_add_u32 m0, %[mx], 42752\n\ts_nop 0\n\tbuffer_load_dwordx4 v49, %[rx], %0 offen lds\n\ts_add_u32 m0, %[mx], 43776\n\ts_nop 0\n\tbuffer_load_dwordx4 v50, %[rx], %0 offen lds\n\t" \
  "s_add_u32 m0, %[mx], 44800\n\ts_nop 0\n\tbuffer_load_dwordx4 v51, %[rx], %0 offen lds\n\ts_add_u32 %0, %0, 128\n\ts_bitset0_b32 %0, 11\n\ts_add_u32 m0, %[mb], 41728\n\t" \
  "s_nop 0\n\tbuffer_load_dwordx4 v52, %[rw], %1 offen lds\n\ts_add_u32 m0, %[mb], 45952\n\ts_nop 0\n\tbuffer_load_dwordx4 v53, %[rw], %1 offen lds\n\ts_add_u32 m0, %[mb], 50176\n\t" \
  "s_nop 0\n\tbuffer_load_dwordx4 v54, %[rw], %1 offen lds\n\ts_add_u32 m0, %[mb], 54400\n\ts_nop 0\n\tbuffer_load_dwordx4 v55, %[rw], %1 offen lds\n\ts_add_u32 m0, %[mb], 58624\n\t" \
  "s_nop 0\n\tbuffer_load_dwordx4 v56, %[rw], %1 offen lds\n\ts_add_u32 m0, %[mb], 62848\n\ts_nop 0\n\tbuffer_load_dwordx4 v57, %[rw], %1 offen lds\n\ts_add_u32 %1, %1, 393216\n\t" \
  "s_sub_u32 vcc_lo, %1, 6291456\n\ts_min_u32 %1, %1, vcc_lo\n\t"
#define LOAD_R \
  "v_mov_b64 v[4:5], 0\n\tv_mov_b64 v[6:7], 0\n\tv_mov_b64 v[8:9], 0\n\tv_mov_b64 v[10:11], 0\n\tbuffer_load_dwordx2 v[38:39], v34, %[rs], 0 offen sc1\n\t" \
  "buffer_load_dword v33, v33, %[rs], 0 offen\n\ts_waitcnt vmcnt(12)\n\ts_barrier\n\ts_mov_b32 %2, 0\n\ts_cmp_lt_u32 %[w], 3\n\ts_cbranch_scc0 .Lr0%=\n\t" \
  "s_call_b64 %[ra], .Lrs%=\n\t.Lr0%=:\n\ts_add_u32 m0, %[mx], 83456\n\ts_nop 0\n\tbuffer_load_dwordx4 v48, %[rx], %0 offen lds\n\ts_add_u32 m0, %[mx], 84480\n\ts_nop 0\n\t" \
  "buffer_load_dwordx4 v49, %[rx], %0 offen lds\n\ts_add_u32 m0, %[mx], 85504\n\ts_nop 0\n\tbuffer_load_dwordx4 v50, %[rx], %0 offen lds\n\ts_add_u32 m0, %[mx], 86528\n\ts_nop 0\n\t" \
  "buffer_load_dwordx4 v51, %[rx], %0 offen lds\n\ts_add_u32 %0, %0, 128\n\ts_bitset0_b32 %0, 11\n\ts_add_u32 m0, %[mb], 83456\n\ts_nop 0\n\t" \
  "buffer_load_dwordx4 v52, %[rw], %1 offen lds\n\ts_add_u32 m0, %[mb], 87680\n\ts_nop 0\n\tbuffer_load_dwordx4 v53, %[rw], %1 offen lds\n\ts_add_u32 m0, %[mb], 91904\n\ts_nop 0\n\t" \
  "buffer_load_dwordx4 v54, %[rw], %1 offen lds\n\ts_add_u32 m0, %[mb], 96128\n\ts_nop 0\n\tbuffer_load_dwordx4 v55, %[rw], %1 offen lds\n\ts_add_u32 m0, %[mb], 100352\n\ts_nop 0\n\t" \
  "buffer_load_dwordx4 v56, %[rw], %1 offen lds\n\ts_add_u32 m0, %[mb], 104576\n\ts_nop 0\n\tbuffer_load_dwordx4 v57, %[rw], %1 offen lds\n\ts_add_u32 %1, %1, 393216\n\t" \
  "s_sub_u32 vcc_lo, %1, 6291456\n\ts_min_u32 %1, %1, vcc_lo\n\ts_waitcnt vmcnt(12)\n\ts_barrier\n\ts_mov_b32 %2, 41728\n\ts_cmp_lt_u32 %[w], 3\n\ts_cbranch_scc0 .Lr1%=\n\t" \
  "s_call_b64 %[ra], .Lrs%=\n\t.Lr1%=:\n\ts_add_u32 m0, %[mx], 0\n\ts_nop 0\n\tbuffer_load_dwordx4 v48, %[rx], %0 offen lds\n\ts_add_u32 m0, %[mx], 1024\n\ts_nop 0\n\t" \
  "buffer_load_dwordx4 v49, %[rx], %0 offen lds\n\ts_add_u32 m0, %[mx], 2048\n\ts_nop 0\n\tbuffer_load_dwordx4 v50, %[rx], %0 offen lds\n\ts_add_u32 m0, %[mx], 3072\n\ts_nop 0\n\t" \
  "buffer_load_dwordx4 v51, %[rx], %0 offen lds\n\ts_add_u32 %0, %0, 128\n\ts_bitset0_b32 %0, 11\n\ts_add_u32 m0, %[mb], 0\n\ts_nop 0\n\t" \
  "buffer_load_dwordx4 v52, %[rw], %1 offen lds\n\ts_add_u32 m0, %[mb], 4224\n\ts_nop 0\n\tbuffer_load_dwordx4 v53, %[rw], %1 offen lds\n\ts_add_u32 m0, %[mb], 8448\n\ts_nop 0\n\t" \
  "buffer_load_dwordx4 v54, %[rw], %1 offen lds\n\ts_add_u32 m0, %[mb], 12672\n\ts_nop 0\n\tbuffer_load_dwordx4 v55, %[rw], %1 offen lds\n\ts_add_u32 m0, %[mb], 16896\n\ts_nop 0\n\t" \
  "buffer_load_dwordx4 v56, %[rw], %1 offen lds\n\ts_add_u32 m0, %[mb], 21120\n\ts_nop 0\n\tbuffer_load_dwordx4 v57, %[rw], %1 offen lds\n\ts_add_u32 %1, %1, 393216\n\t" \
  "s_sub_u32 vcc_lo, %1, 6291456\n\ts_min_u32 %1, %1, vcc_lo\n\ts_waitcnt vmcnt(10)\n\ts_barrier\n\ts_mov_b32 %2, 41728\n\ts_cmp_lt_u32 %[w], 3\n\ts_cbranch_scc0 .Lr2%=\n\t" \
  "s_call_b64 %[ra], .Lrs%=\n\t.Lr2%=:\n\ts_add_u32 m0, %[mx], 41728\n\ts_nop 0\n\tbuffer_load_dwordx4 v48, %[rx], %0 offen lds\n\ts_add_u32 m0, %[mx], 42752\n\ts_nop 0\n\t" \
  "buffer_load_dwordx4 v49, %[rx], %0 offen lds\n\ts_add_u32 m0, %[mx], 43776\n\ts_nop 0\n\tbuffer_load_dwordx4 v50, %[rx], %0 offen lds\n\ts_add_u32 m0, %[mx], 44800\n\ts_nop 0\n\t" \
  "buffer_load_dwordx4 v51, %[rx], %0 offen lds\n\ts_add_u32 %0, %0, 128\n\ts_bitset0_b32 %0, 11\n\ts_add_u32 m0, %[mb], 41728\n\ts_nop 0\n\t" \
  "buffer_load_dwordx4 v52, %[rw], %1 offen lds\n\ts_add_u32 m0, %[mb], 45952\n\ts_nop 0\n\tbuffer_load_dwordx4 v53, %[rw], %1 offen lds\n\ts_add_u32 m0, %[mb], 50176\n\ts_nop 0\n\t" \
  "buffer_load_dwordx4 v54, %[rw], %1 offen lds\n\ts_add_u32 m0, %[mb], 54400\n\ts_nop 0\n\tbuffer_load_dwordx4 v55, %[rw], %1 offen lds\n\ts_add_u32 m0, %[mb], 58624\n\ts_nop 0\n\t" \
  "buffer_load_dwordx4 v56, %[rw], %1 offen lds\n\ts_add_u32 m0, %[mb], 62848\n\ts_nop 0\n\tbuffer_load_dwordx4 v57, %[rw], %1 offen lds\n\ts_add_u32 %1, %1, 393216\n\t" \
  "s_sub_u32 vcc_lo, %1, 6291456\n\ts_min_u32 %1, %1, vcc_lo\n\ts_waitcnt vmcnt(10)\n\ts_barrier\n\ts_mov_b32 %2, -83456\n\ts_cmp_lt_u32 %[w], 3\n\ts_cbranch_scc0 .Lr3%=\n\t" \
  "s_call_b64 %[ra], .Lrs%=\n\t.Lr3%=:\n\ts_add_u32 m0, %[mx], 83456\n\ts_nop 0\n\tbuffer_load_dwordx4 v48, %[rx], %0 offen lds\n\ts_add_u32 m0, %[mx], 84480\n\ts_nop 0\n\t" \
  "buffer_load_dwordx4 v49, %[rx], %0 offen lds\n\ts_add_u32 m0, %[mx], 85504\n\ts_nop 0\n\tbuffer_load_dwordx4 v50, %[rx], %0 offen lds\n\ts_add_u32 m0, %[mx], 86528\n\ts_nop 0\n\t" \
  "buffer_load_dwordx4 v51, %[rx], %0 offen lds\n\ts_add_u32 %0, %0, 128\n\ts_bitset0_b32 %0, 11\n\ts_add_u32 m0, %[mb], 83456\n\ts_nop 0\n\t" \
  "buffer_load_dwordx4 v52, %[rw], %1 offen lds\n\ts_add_u32 m0, %[mb], 87680\n\ts_nop 0\n\tbuffer_load_dwordx4 v53, %[rw], %1 offen lds\n\ts_add_u32 m0, %[mb], 91904\n\ts_nop 0\n\t" \
  "buffer_load_dwordx4 v54, %[rw], %1 offen lds\n\ts_add_u32 m0, %[mb], 96128\n\ts_nop 0\n\tbuffer_load_dwordx4 v55, %[rw], %1 offen lds\n\ts_add_u32 m0, %[mb], 100352\n\ts_nop 0\n\t" \
  "buffer_load_dwordx4 v56, %[rw], %1 offen lds\n\ts_add_u32 m0, %[mb], 104576\n\ts_nop 0\n\tbuffer_load_dwordx4 v57, %[rw], %1 offen lds\n\ts_add_u32 %1, %1, 393216\n\t" \
  "s_sub_u32 vcc_lo, %1, 6291456\n\ts_min_u32 %1, %1, vcc_lo\n\ts_waitcnt vmcnt(10)\n\ts_barrier\n\ts_add_u32 m0, %[mx], 0\n\t" \
  "v_add_f32 v40, v8, v9\n\tv_add_f32 v42, v10, v11\n\tv_add_f32_dpp v44, v4, v4 row_ror:8 row_mask:0x3 bank_mask:0x3\n\tv_add_f32_dpp v44, v5, v5 row_ror:8 row_mask:0x3 bank_mask:0xc\n\tv_add_f32_dpp v44, v6, v6 row_ror:8 row_mask:0xc bank_mask:0x3\n\tv_add_f32_dpp v44, v7, v7 row_ror:8 row_mask:0xc bank_mask:0xc\n\t" \
  "buffer_load_dwordx4 v48, %[rx], %0 offen lds\n\ts_add_u32 m0, %[mx], 1024\n\tv_permlane32_swap_b32 v40, v42\n\tbuffer_load_dwordx4 v49, %[rx], %0 offen lds\n\t" \
  "s_add_u32 m0, %[mx], 2048\n\tv_add_f32 v40, v40, v42\n\tv_mov_b32 v42, v40\n\tbuffer_load_dwordx4 v50, %[rx], %0 offen lds\n\ts_add_u32 m0, %[mx], 3072\n\t" \
  "v_permlane16_swap_b32 v40, v42\n\tbuffer_load_dwordx4 v51, %[rx], %0 offen lds\n\ts_add_u32 %0, %0, 128\n\ts_bitset0_b32 %0, 11\n\ts_add_u32 m0, %[mb], 0\n\t" \
  "v_add_u32 v41, 0x7f4a7c15, v38\n\tv_add_f32 v40, v40, v42\n\tv_add_f32 v40, v40, v44\n\tbuffer_load_dwordx4 v52, %[rw], %1 offen lds\n\ts_add_u32 m0, %[mb], 4224\n\t" \
  "v_add_u32 v39, 0x9e3779b9, v39\n\tds_write2_b32 v45, v41, v39 offset1:1\n\tbuffer_load_dwordx4 v53, %[rw], %1 offen lds\n\ts_add_u32 m0, %[mb], 8448\n\ts_nop 0\n\t" \
  "buffer_load_dwordx4 v54, %[rw], %1 offen lds\n\ts_add_u32 m0, %[mb], 12672\n\ts_nop 0\n\tbuffer_load_dwordx4 v55, %[rw], %1 offen lds\n\ts_add_u32 m0, %[mb], 16896\n\ts_nop 0\n\t" \
  "buffer_load_dwordx4 v56, %[rw], %1 offen lds\n\ts_add_u32 m0, %[mb], 21120\n\ts_nop 0\n\tbuffer_load_dwordx4 v57, %[rw], %1 offen lds\n\ts_add_u32 %1, %1, 393216\n\t" \
  "s_sub_u32 vcc_lo, %1, 6291456\n\ts_min_u32 %1, %1, vcc_lo\n\tbuffer_store_dwordx2 v[40:41], v37, %[rs], 0 offen sc1\n\ts_waitcnt vmcnt(11)\n\ts_barrier\n\t" \
  "s_mov_b32 %2, 2\n\t.Lld%=:\n\ts_add_u32 m0, %[mx], 41728\n\ts_nop 0\n\tbuffer_load_dwordx4 v48, %[rx], %0 offen lds\n\ts_add_u32 m0, %[mx], 42752\n\ts_nop 0\n\t" \
  "buffer_load_dwordx4 v49, %[rx], %0 offen lds\n\ts_add_u32 m0, %[mx], 43776\n\ts_nop 0\n\tbuffer_load_dwordx4 v50, %[rx], %0 offen lds\n\ts_add_u32 m0, %[mx], 44800\n\ts_nop 0\n\t" \
  "buffer_load_dwordx4 v51, %[rx], %0 offen lds\n\ts_add_u32 %0, %0, 128\n\ts_bitset0_b32 %0, 11\n\ts_add_u32 m0, %[mb], 41728\n\ts_nop 0\n\t" \
  "buffer_load_dwordx4 v52, %[rw], %1 offen lds\n\ts_add_u32 m0, %[mb], 45952\n\ts_nop 0\n\tbuffer_load_dwordx4 v53, %[rw], %1 offen lds\n\ts_add_u32 m0, %[mb], 50176\n\ts_nop 0\n\t" \
  "buffer_load_dwordx4 v54, %[rw], %1 offen lds\n\ts_add_u32 m0, %[mb], 54400\n\ts_nop 0\n\tbuffer_load_dwordx4 v55, %[rw], %1 offen lds\n\ts_add_u32 m0, %[mb], 58624\n\ts_nop 0\n\t" \
  "buffer_load_dwordx4 v56, %[rw], %1 offen lds\n\ts_add_u32 m0, %[mb], 62848\n\ts_nop 0\n\tbuffer_load_dwordx4 v57, %[rw], %1 offen lds\n\ts_add_u32 %1, %1, 393216\n\t" \
  "s_sub_u32 vcc_lo, %1, 6291456\n\ts_min_u32 %1, %1, vcc_lo\n\ts_waitcnt vmcnt(10)\n\ts_barrier\n\ts_add_u32 m0, %[mx], 83456\n\ts_nop 0\n\t" \
  "buffer_load_dwordx4 v48, %[rx], %0 offen lds\n\ts_add_u32 m0, %[mx], 84480\n\ts_nop 0\n\tbuffer_load_dwordx4 v49, %[rx], %0 offen lds\n\ts_add_u32 m0, %[mx], 85504\n\ts_nop 0\n\t" \
  "buffer_load_dwordx4 v50, %[rx], %0 offen lds\n\ts_add_u32 m0, %[mx], 86528\n\ts_nop 0\n\tbuffer_load_dwordx4 v51, %[rx], %0 offen lds\n\ts_add_u32 %0, %0, 128\n\t" \
  "s_bitset0_b32 %0, 11\n\ts_add_u32 m0, %[mb], 83456\n\ts_nop 0\n\tbuffer_load_dwordx4 v52, %[rw], %1 offen lds\n\ts_add_u32 m0, %[mb], 87680\n\ts_nop 0\n\t" \
  "buffer_load_dwordx4 v53, %[rw], %1 offen lds\n\ts_add_u32 m0, %[mb], 91904\n\ts_nop 0\n\tbuffer_load_dwordx4 v54, %[rw], %1 offen lds\n\ts_add_u32 m0, %[mb], 96128\n\ts_nop 0\n\t" \
  "buffer_load_dwordx4 v55, %[rw], %1 offen lds\n\ts_add_u32 m0, %[mb], 100352\n\ts_nop 0\n\tbuffer_load_dwordx4 v56, %[rw], %1 offen lds\n\ts_add_u32 m0, %[mb], 104576\n\ts_nop 0\n\t" \
  "buffer_load_dwordx4 v57, %[rw], %1 offen lds\n\ts_add_u32 %1, %1, 393216\n\ts_sub_u32 vcc_lo, %1, 6291456\n\ts_min_u32 %1, %1, vcc_lo\n\ts_waitcnt vmcnt(10)\n\ts_barrier\n\t" \
  "s_add_u32 m0, %[mx], 0\n\ts_nop 0\n\tbuffer_load_dwordx4 v48, %[rx], %0 offen lds\n\ts_add_u32 m0, %[mx], 1024\n\ts_nop 0\n\tbuffer_load_dwordx4 v49, %[rx], %0 offen lds\n\t" \
  "s_add_u32 m0, %[mx], 2048\n\ts_nop 0\n\tbuffer_load_dwordx4 v50, %[rx], %0 offen lds\n\ts_add_u32 m0, %[mx], 3072\n\ts_nop 0\n\tbuffer_load_dwordx4 v51, %[rx], %0 offen lds\n\t" \
  "s_add_u32 %0, %0, 128\n\ts_bitset0_b32 %0, 11\n\ts_add_u32 m0, %[mb], 0\n\ts_nop 0\n\tbuffer_load_dwordx4 v52, %[rw], %1 offen lds\n\ts_add_u32 m0, %[mb], 4224\n\ts_nop 0\n\t" \
  "buffer_load_dwordx4 v53, %[rw], %1 offen lds\n\ts_add_u32 m0, %[mb], 8448\n\ts_nop 0\n\tbuffer_load_dwordx4 v54, %[rw], %1 offen lds\n\ts_add_u32 m0, %[mb], 12672\n\ts_nop 0\n\t" \
  "buffer_load_dwordx4 v55, %[rw], %1 offen lds\n\ts_add_u32 m0, %[mb], 16896\n\ts_nop 0\n\tbuffer_load_dwordx4 v56, %[rw], %1 offen lds\n\ts_add_u32 m0, %[mb], 21120\n\ts_nop 0\n\t" \
  "buffer_load_dwordx4 v57, %[rw], %1 offen lds\n\ts_add_u32 %1, %1, 393216\n\ts_sub_u32 vcc_lo, %1, 6291456\n\ts_min_u32 %1, %1, vcc_lo\n\ts_sub_u32 %2, %2, 1\n\t" \
  "s_cmp_lg_u32 %2, 0\n\ts_waitcnt vmcnt(10)\n\ts_barrier\n\ts_cbranch_scc1 .Lld%=\n\ts_add_u32 m0, %[mx], 41728\n\ts_nop 0\n\tbuffer_load_dwordx4 v48, %[rx], %0 offen lds\n\t" \
  "s_add_u32 m0, %[mx], 42752\n\ts_nop 0\n\tbuffer_load_dwordx4 v49, %[rx], %0 offen lds\n\ts_add_u32 m0, %[mx], 43776\n\ts_nop 0\n\tbuffer_load_dwordx4 v50, %[rx], %0 offen lds\n\t" \
  "s_add_u32 m0, %[mx], 44800\n\ts_nop 0\n\tbuffer_load_dwordx4 v51, %[rx], %0 offen lds\n\ts_add_u32 %0, %0, 128\n\ts_bitset0_b32 %0, 11\n\ts_add_u32 m0, %[mb], 41728\n\t" \
  "s_nop 0\n\tbuffer_load_dwordx4 v52, %[rw], %1 offen lds\n\ts_add_u32 m0, %[mb], 45952\n\ts_nop 0\n\tbuffer_load_dwordx4 v53, %[rw], %1 offen lds\n\ts_add_u32 m0, %[mb], 50176\n\t" \
  "s_nop 0\n\tbuffer_load_dwordx4 v54, %[rw], %1 offen lds\n\ts_add_u32 m0, %[mb], 54400\n\ts_nop 0\n\tbuffer_load_dwordx4 v55, %[rw], %1 offen lds\n\ts_add_u32 m0, %[mb], 58624\n\t" \
  "s_nop 0\n\tbuffer_load_dwordx4 v56, %[rw], %1 offen lds\n\ts_add_u32 m0, %[mb], 62848\n\ts_nop 0\n\tbuffer_load_dwordx4 v57, %[rw], %1 offen lds\n\ts_add_u32 %1, %1, 393216\n\t" \
  "s_sub_u32 vcc_lo, %1, 6291456\n\ts_min_u32 %1, %1, vcc_lo\n\tbuffer_load_dwordx4 v[8:11], v58, %[rs], 0 offen offset:0 sc1\n\t" \
  "buffer_load_dwordx4 v[12:15], v58, %[rs], 0 offen offset:512 sc1\n\tbuffer_load_dwordx2 v[0:1], v63, %[rs], 0 offen offset:0 sc1\n\t" \
  "buffer_load_dwordx2 v[2:3], v63, %[rs], 0 offen offset:512 sc1\n\tbuffer_load_dwordx2 v[4:5], v63, %[rs], 0 offen offset:1024 sc1\n\t" \
  "buffer_load_dwordx2 v[6:7], v63, %[rs], 0 offen offset:1536 sc1\n\ts_waitcnt vmcnt(16)\n\ts_barrier\n\ts_add_u32 m0, %[mx], 83456\n\ts_nop 0\n\t" \
  "buffer_load_dwordx4 v48, %[rx], %0 offen lds\n\ts_add_u32 m0, %[mx], 84480\n\ts_nop 0\n\tbuffer_load_dwordx4 v49, %[rx], %0 offen lds\n\ts_add_u32 m0, %[mx], 85504\n\ts_nop 0\n\t" \
  "buffer_load_dwordx4 v50, %[rx], %0 offen lds\n\ts_add_u32 m0, %[mx], 86528\n\ts_nop 0\n\tbuffer_load_dwordx4 v51, %[rx], %0 offen lds\n\ts_add_u32 %0, %0, 128\n\t" \
  "s_bitset0_b32 %0, 11\n\ts_add_u32 m0, %[mb], 83456\n\ts_nop 0\n\tbuffer_load_dwordx4 v52, %[rw], %1 offen lds\n\ts_add_u32 m0, %[mb], 87680\n\ts_nop 0\n\t" \
  "buffer_load_dwordx4 v53, %[rw], %1 offen lds\n\ts_add_u32 m0, %[mb], 91904\n\ts_nop 0\n\tbuffer_load_dwordx4 v54, %[rw], %1 offen lds\n\ts_add_u32 m0, %[mb], 96128\n\ts_nop 0\n\t" \
  "buffer_load_dwordx4 v55, %[rw], %1 offen lds\n\ts_add_u32 m0, %[mb], 100352\n\ts_nop 0\n\tbuffer_load_dwordx4 v56, %[rw], %1 offen lds\n\ts_add_u32 m0, %[mb], 104576\n\ts_nop 0\n\t" \
  "buffer_load_dwordx4 v57, %[rw], %1 offen lds\n\ts_add_u32 %1, %1, 393216\n\ts_sub_u32 vcc_lo, %1, 6291456\n\ts_min_u32 %1, %1, vcc_lo\n\ts_waitcnt vmcnt(16)\n\ts_barrier\n\t" \
  "s_add_u32 m0, %[mx], 0\n\ts_nop 0\n\tbuffer_load_dwordx4 v48, %[rx], %0 offen lds\n\ts_add_u32 m0, %[mx], 1024\n\ts_nop 0\n\tbuffer_load_dwordx4 v49, %[rx], %0 offen lds\n\t" \
  "s_add_u32 m0, %[mx], 2048\n\ts_nop 0\n\tbuffer_load_dwordx4 v50, %[rx], %0 offen lds\n\ts_add_u32 m0, %[mx], 3072\n\ts_nop 0\n\tbuffer_load_dwordx4 v51, %[rx], %0 offen lds\n\t" \
  "s_add_u32 %0, %0, 128\n\ts_bitset0_b32 %0, 11\n\ts_add_u32 m0, %[mb], 0\n\ts_nop 0\n\tbuffer_load_dwordx4 v52, %[rw], %1 offen lds\n\ts_add_u32 m0, %[mb], 4224\n\ts_nop 0\n\t" \
  "buffer_load_dwordx4 v53, %[rw], %1 offen lds\n\ts_add_u32 m0, %[mb], 8448\n\ts_nop 0\n\tbuffer_load_dwordx4 v54, %[rw], %1 offen lds\n\ts_add_u32 m0, %[mb], 12672\n\ts_nop 0\n\t" \
  "buffer_load_dwordx4 v55, %[rw], %1 offen lds\n\ts_add_u32 m0, %[mb], 16896\n\ts_nop 0\n\tbuffer_load_dwordx4 v56, %[rw], %1 offen lds\n\ts_add_u32 m0, %[mb], 21120\n\ts_nop 0\n\t" \
  "buffer_load_dwordx4 v57, %[rw], %1 offen lds\n\ts_add_u32 %1, %1, 393216\n\ts_sub_u32 vcc_lo, %1, 6291456\n\ts_min_u32 %1, %1, vcc_lo\n\ts_waitcnt vmcnt(10)\n\ts_barrier\n\t" \
  "s_branch .Lre%=\n\t.Lrs%=:\n\tv_add_u32 v35, %2, v35\n\ts_bitcmp1_b32 %[w], 0\n\ts_cbranch_scc1 .Lrc%=\n\tds_read_b128 v[64:67], v35 offset:0\n\t" \
  "ds_read_b128 v[68:71], v35 offset:1024\n\ts_waitcnt lgkmcnt(1)\n\tv_lshlrev_b32 v72, 16, v64\n\tv_and_b32 v73, v46, v64\n\tv_lshlrev_b32 v74, 16, v65\n\t" \
  "v_and_b32 v75, v46, v65\n\tv_lshlrev_b32 v76, 16, v66\n\tv_and_b32 v77, v46, v66\n\tv_lshlrev_b32 v78, 16, v67\n\tv_and_b32 v79, v46, v67\n\t" \
  "v_pk_add_f32 v[8:9], v[8:9], v[72:73]\n\tv_pk_fma_f32 v[10:11], v[72:73], v[72:73], v[10:11]\n\tv_pk_add_f32 v[8:9], v[8:9], v[74:75]\n\t" \
  "v_pk_fma_f32 v[10:11], v[74:75], v[74:75], v[10:11]\n\tv_pk_add_f32 v[8:9], v[8:9], v[76:77]\n\tv_pk_fma_f32 v[10:11], v[76:77], v[76:77], v[10:11]\n\t" \
  "v_pk_add_f32 v[8:9], v[8:9], v[78:79]\n\tv_pk_fma_f32 v[10:11], v[78:79], v[78:79], v[10:11]\n\ts_waitcnt lgkmcnt(0)\n\tv_lshlrev_b32 v80, 16, v68\n\t" \
  "v_and_b32 v81, v46, v68\n\tv_lshlrev_b32 v82, 16, v69\n\tv_and_b32 v83, v46, v69\n\tv_lshlrev_b32 v84, 16, v70\n\tv_and_b32 v85, v46, v70\n\t" \
  "v_lshlrev_b32 v86, 16, v71\n\tv_and_b32 v87, v46, v71\n\tv_pk_add_f32 v[8:9], v[8:9], v[80:81]\n\tv_pk_fma_f32 v[10:11], v[80:81], v[80:81], v[10:11]\n\t" \
  "v_pk_add_f32 v[8:9], v[8:9], v[82:83]\n\tv_pk_fma_f32 v[10:11], v[82:83], v[82:83], v[10:11]\n\tv_pk_add_f32 v[8:9], v[8:9], v[84:85]\n\t" \
  "v_pk_fma_f32 v[10:11], v[84:85], v[84:85], v[10:11]\n\tv_pk_add_f32 v[8:9], v[8:9], v[86:87]\n\tv_pk_fma_f32 v[10:11], v[86:87], v[86:87], v[10:11]\n\t" \
  "s_setpc_b64 %[ra]\n\t.Lrc%=:\n\tds_read_b128 v[64:67], v35 offset:0\n\tds_read_b128 v[68:71], v35 offset:1056\n\tds_read_b128 v[72:75], v35 offset:2112\n\t" \
  "ds_read_b128 v[76:79], v35 offset:3168\n\tds_read_b128 v[80:83], v35 offset:4224\n\tds_read_b128 v[84:87], v35 offset:5280\n\tds_read_b128 v[88:91], v35 offset:6336\n\t" \
  "ds_read_b128 v[92:95], v35 offset:7392\n\ts_waitcnt lgkmcnt(7)\n\tv_mfma_f32_16x16x32_bf16 v[4:7], v[0:3], v[64:67], v[4:7]\n\ts_waitcnt lgkmcnt(6)\n\t" \
  "v_mfma_f32_16x16x32_bf16 v[4:7], v[0:3], v[68:71], v[4:7]\n\ts_waitcnt lgkmcnt(5)\n\tv_mfma_f32_16x16x32_bf16 v[4:7], v[0:3], v[72:75], v[4:7]\n\ts_waitcnt lgkmcnt(4)\n\t" \
  "v_mfma_f32_16x16x32_bf16 v[4:7], v[0:3], v[76:79], v[4:7]\n\ts_waitcnt lgkmcnt(3)\n\tv_mfma_f32_16x16x32_bf16 v[4:7], v[0:3], v[80:83], v[4:7]\n\ts_waitcnt lgkmcnt(2)\n\t" \
  "v_mfma_f32_16x16x32_bf16 v[4:7], v[0:3], v[84:87], v[4:7]\n\ts_waitcnt lgkmcnt(1)\n\tv_mfma_f32_16x16x32_bf16 v[4:7], v[0:3], v[88:91], v[4:7]\n\ts_waitcnt lgkmcnt(0)\n\t" \
  "v_mfma_f32_16x16x32_bf16 v[4:7], v[0:3], v[92:95], v[4:7]\n\ts_setpc_b64 %[ra]\n\t.Lre%=:\n\t"
#define LOAD_Q0 \
  "s_nop 4\n\ts_add_u32 m0, %[mb], 0\n\ts_nop 0\n\tbuffer_load_dwordx4 v52, %[rw], %1 offen lds\n\ts_add_u32 m0, %[mb], 4224\n\ts_nop 0\n\t" \
  "buffer_load_dwordx4 v53, %[rw], %1 offen lds\n\ts_add_u32 m0, %[mb], 8448\n\ts_nop 0\n\tbuffer_load_dwordx4 v54, %[rw], %1 offen lds\n\ts_add_u32 m0, %[mb], 12672\n\ts_nop 0\n\t" \
  "buffer_load_dwordx4 v55, %[rw], %1 offen lds\n\ts_add_u32 m0, %[mb], 16896\n\ts_nop 0\n\tbuffer_load_dwordx4 v56, %[rw], %1 offen lds\n\ts_add_u32 m0, %[mb], 21120\n\ts_nop 0\n\t" \
  "buffer_load_dwordx4 v57, %[rw], %1 offen lds\n\ts_add_u32 %1, %1, 393216\n\ts_sub_u32 vcc_lo, %1, 6291456\n\ts_min_u32 %1, %1, vcc_lo\n\ts_barrier\n\t" \
  "s_add_u32 m0, %[mb], 41728\n\ts_nop 0\n\tbuffer_load_dwordx4 v52, %[rw], %1 offen lds\n\ts_add_u32 m0, %[mb], 45952\n\ts_nop 0\n\tbuffer_load_dwordx4 v53, %[rw], %1 offen lds\n\t" \
  "s_add_u32 m0, %[mb], 50176\n\ts_nop 0\n\tbuffer_load_dwordx4 v54, %[rw], %1 offen lds\n\ts_add_u32 m0, %[mb], 54400\n\ts_nop 0\n\tbuffer_load_dwordx4 v55, %[rw], %1 offen lds\n\t" \
  "s_add_u32 m0, %[mb], 58624\n\ts_nop 0\n\tbuffer_load_dwordx4 v56, %[rw], %1 offen lds\n\ts_add_u32 m0, %[mb], 62848\n\ts_nop 0\n\tbuffer_load_dwordx4 v57, %[rw], %1 offen lds\n\t" \
  "s_add_u32 %1, %1, 393216\n\ts_sub_u32 vcc_lo, %1, 6291456\n\ts_min_u32 %1, %1, vcc_lo\n\ts_add_u32 m0, %[mb], 83456\n\ts_nop 0\n\t" \
  "buffer_load_dwordx4 v52, %[rw], %1 offen lds\n\ts_add_u32 m0, %[mb], 87680\n\ts_nop 0\n\tbuffer_load_dwordx4 v53, %[rw], %1 offen lds\n\ts_add_u32 m0, %[mb], 91904\n\ts_nop 0\n\t" \
  "buffer_load_dwordx4 v54, %[rw], %1 offen lds\n\ts_add_u32 m0, %[mb], 96128\n\ts_nop 0\n\tbuffer_load_dwordx4 v55, %[rw], %1 offen lds\n\ts_add_u32 m0, %[mb], 100352\n\ts_nop 0\n\t" \
  "buffer_load_dwordx4 v56, %[rw], %1 offen lds\n\ts_add_u32 m0, %[mb], 104576\n\ts_nop 0\n\tbuffer_load_dwordx4 v57, %[rw], %1 offen lds\n\ts_add_u32 %1, %1, 393216\n\t" \
  "s_sub_u32 vcc_lo, %1, 6291456\n\ts_min_u32 %1, %1, vcc_lo\n\t"
#define LOAD_Q \
  "s_add_u32 m0, %[mx], 0\n\ts_nop 0\n\tbuffer_load_dwordx4 v48, %[rx], %0 offen offset:0 sc1 lds\n\tbuffer_load_dwordx4 v48, %[rx], %0 offen offset:1024 sc1 lds\n\t" \
  "buffer_load_dwordx4 v48, %[rx], %0 offen offset:2048 sc1 lds\n\tbuffer_load_dwordx4 v48, %[rx], %0 offen offset:3072 sc1 lds\n\ts_add_u32 %0, %0, 16384\n\t" \
  "s_bitset0_b32 %0, 18\n\ts_add_u32 m0, %[mx], 41728\n\ts_nop 0\n\tbuffer_load_dwordx4 v48, %[rx], %0 offen offset:0 sc1 lds\n\t" \
  "buffer_load_dwordx4 v48, %[rx], %0 offen offset:1024 sc1 lds\n\tbuffer_load_dwordx4 v48, %[rx], %0 offen offset:2048 sc1 lds\n\t" \
  "buffer_load_dwordx4 v48, %[rx], %0 offen offset:3072 sc1 lds\n\ts_add_u32 %0, %0, 16384\n\ts_bitset0_b32 %0, 18\n\ts_add_u32 m0, %[mx], 83456\n\ts_nop 0\n\t" \
  "buffer_load_dwordx4 v48, %[rx], %0 offen offset:0 sc1 lds\n\tbuffer_load_dwordx4 v48, %[rx], %0 offen offset:1024 sc1 lds\n\t" \
  "buffer_load_dwordx4 v48, %[rx], %0 offen offset:2048 sc1 lds\n\tbuffer_load_dwordx4 v48, %[rx], %0 offen offset:3072 sc1 lds\n\ts_add_u32 %0, %0, 16384\n\t" \
  "s_bitset0_b32 %0, 18\n\ts_waitcnt vmcnt(8)\n\ts_barrier\n\tbuffer_load_dwordx4 v[0:3], v49, %[rs], 0 offen offset:0 sc1\n\t" \
  "buffer_load_dwordx4 v[4:7], v49, %[rs], 0 offen offset:512 sc1\n\tbuffer_load_dwordx4 v[8:11], v49, %[rs], 0 offen offset:1024 sc1\n\t" \
  "buffer_load_dwordx4 v[12:15], v49, %[rs], 0 offen offset:1536 sc1\n\tbuffer_load_dwordx4 v[16:19], v49, %[rs], 0 offen offset:2048 sc1\n\t" \
  "buffer_load_dwordx4 v[20:23], v49, %[rs], 0 offen offset:2560 sc1\n\tbuffer_load_dwordx4 v[24:27], v49, %[rs], 0 offen offset:3072 sc1\n\t" \
  "buffer_load_dwordx4 v[28:31], v49, %[rs], 0 offen offset:3584 sc1\n\ts_waitcnt vmcnt(12)\n\ts_barrier\n\ts_add_u32 m0, %[mx], 0\n\ts_nop 0\n\t" \
  "buffer_load_dwordx4 v48, %[rx], %0 offen offset:0 sc1 lds\n\tbuffer_load_dwordx4 v48, %[rx], %0 offen offset:1024 sc1 lds\n\t" \
  "buffer_load_dwordx4 v48, %[rx], %0 offen offset:2048 sc1 lds\n\tbuffer_load_dwordx4 v48, %[rx], %0 offen offset:3072 sc1 lds\n\ts_add_u32 %0, %0, 16384\n\t" \
  "s_bitset0_b32 %0, 18\n\ts_add_u32 m0, %[mb], 0\n\ts_nop 0\n\tbuffer_load_dwordx4 v52, %[rw], %1 offen lds\n\ts_add_u32 m0, %[mb], 4224\n\ts_nop 0\n\t" \
  "buffer_load_dwordx4 v53, %[rw], %1 offen lds\n\ts_add_u32 m0, %[mb], 8448\n\ts_nop 0\n\tbuffer_load_dwordx4 v54, %[rw], %1 offen lds\n\ts_add_u32 m0, %[mb], 12672\n\ts_nop 0\n\t" \
  "buffer_load_dwordx4 v55, %[rw], %1 offen lds\n\ts_add_u32 m0, %[mb], 16896\n\ts_nop 0\n\tbuffer_load_dwordx4 v56, %[rw], %1 offen lds\n\ts_add_u32 m0, %[mb], 21120\n\ts_nop 0\n\t" \
  "buffer_load_dwordx4 v57, %[rw], %1 offen lds\n\ts_add_u32 %1, %1, 393216\n\ts_sub_u32 vcc_lo, %1, 6291456\n\ts_min_u32 %1, %1, vcc_lo\n\ts_waitcnt vmcnt(18)\n\ts_barrier\n\t" \
  "s_mov_b32 %2, 3\n\t.Lld%=:\n\ts_add_u32 m0, %[mx], 41728\n\ts_nop 0\n\tbuffer_load_dwordx4 v48, %[rx], %0 offen offset:0 sc1 lds\n\t" \
  "buffer_load_dwordx4 v48, %[rx], %0 offen offset:1024 sc1 lds\n\tbuffer_load_dwordx4 v48, %[rx], %0 offen offset:2048 sc1 lds\n\t" \
  "buffer_load_dwordx4 v48, %[rx], %0 offen offset:3072 sc1 lds\n\ts_add_u32 %0, %0, 16384\n\ts_bitset0_b32 %0, 18\n\ts_add_u32 m0, %[mb], 41728\n\ts_nop 0\n\t" \
  "buffer_load_dwordx4 v52, %[rw], %1 offen lds\n\ts_add_u32 m0, %[mb], 45952\n\ts_nop 0\n\tbuffer_load_dwordx4 v53, %[rw], %1 offen lds\n\ts_add_u32 m0, %[mb], 50176\n\ts_nop 0\n\t" \
  "buffer_load_dwordx4 v54, %[rw], %1 offen lds\n\ts_add_u32 m0, %[mb], 54400\n\ts_nop 0\n\tbuffer_load_dwordx4 v55, %[rw], %1 offen lds\n\ts_add_u32 m0, %[mb], 58624\n\ts_nop 0\n\t" \
  "buffer_load_dwordx4 v56, %[rw], %1 offen lds\n\ts_add_u32 m0, %[mb], 62848\n\ts_nop 0\n\tbuffer_load_dwordx4 v57, %[rw], %1 offen lds\n\ts_add_u32 %1, %1, 393216\n\t" \
  "s_sub_u32 vcc_lo, %1, 6291456\n\ts_min_u32 %1, %1, vcc_lo\n\ts_waitcnt vmcnt(10)\n\ts_barrier\n\ts_add_u32 m0, %[mx], 83456\n\ts_nop 0\n\t" \
  "buffer_load_dwordx4 v48, %[rx], %0 offen offset:0 sc1 lds\n\tbuffer_load_dwordx4 v48, %[rx], %0 offen offset:1024 sc1 lds\n\t" \
  "buffer_load_dwordx4 v48, %[rx], %0 offen offset:2048 sc1 lds\n\tbuffer_load_dwordx4 v48, %[rx], %0 offen offset:3072 sc1 lds\n\ts_add_u32 %0, %0, 16384\n\t" \
  "s_bitset0_b32 %0, 18\n\ts_add_u32 m0, %[mb], 83456\n\ts_nop 0\n\tbuffer_load_dwordx4 v52, %[rw], %1 offen lds\n\ts_add_u32 m0, %[mb], 87680\n\ts_nop 0\n\t" \
  "buffer_load_dwordx4 v53, %[rw], %1 offen lds\n\ts_add_u32 m0, %[mb], 91904\n\ts_nop 0\n\tbuffer_load_dwordx4 v54, %[rw], %1 offen lds\n\ts_add_u32 m0, %[mb], 96128\n\ts_nop 0\n\t" \
  "buffer_load_dwordx4 v55, %[rw], %1 offen lds\n\ts_add_u32 m0, %[mb], 100352\n\ts_nop 0\n\tbuffer_load_dwordx4 v56, %[rw], %1 offen lds\n\ts_add_u32 m0, %[mb], 104576\n\ts_nop 0\n\t" \
  "buffer_load_dwordx4 v57, %[rw], %1 offen lds\n\ts_add_u32 %1, %1, 393216\n\ts_sub_u32 vcc_lo, %1, 6291456\n\ts_min_u32 %1, %1, vcc_lo\n\ts_waitcnt vmcnt(10)\n\ts_barrier\n\t" \
  "s_add_u32 m0, %[mx], 0\n\ts_nop 0\n\tbuffer_load_dwordx4 v48, %[rx], %0 offen offset:0 sc1 lds\n\tbuffer_load_dwordx4 v48, %[rx], %0 offen offset:1024 sc1 lds\n\t" \
  "buffer_load_dwordx4 v48, %[rx], %0 offen offset:2048 sc1 lds\n\tbuffer_load_dwordx4 v48, %[rx], %0 offen offset:3072 sc1 lds\n\ts_add_u32 %0, %0, 16384\n\t" \
  "s_bitset0_b32 %0, 18\n\ts_add_u32 m0, %[mb], 0\n\ts_nop 0\n\tbuffer_load_dwordx4 v52, %[rw], %1 offen lds\n\ts_add_u32 m0, %[mb], 4224\n\ts_nop 0\n\t" \
  "buffer_load_dwordx4 v53, %[rw], %1 offen lds\n\ts_add_u32 m0, %[mb], 8448\n\ts_nop 0\n\tbuffer_load_dwordx4 v54, %[rw], %1 offen lds\n\ts_add_u32 m0, %[mb], 12672\n\ts_nop 0\n\t" \
  "buffer_load_dwordx4 v55, %[rw], %1 offen lds\n\ts_add_u32 m0, %[mb], 16896\n\ts_nop 0\n\tbuffer_load_dwordx4 v56, %[rw], %1 offen lds\n\ts_add_u32 m0, %[mb], 21120\n\ts_nop 0\n\t" \
  "buffer_load_dwordx4 v57, %[rw], %1 offen lds\n\ts_add_u32 %1, %1, 393216\n\ts_sub_u32 vcc_lo, %1, 6291456\n\ts_min_u32 %1, %1, vcc_lo\n\ts_sub_u32 %2, %2, 1\n\t" \
  "s_cmp_lg_u32 %2, 0\n\ts_waitcnt vmcnt(10)\n\ts_barrier\n\ts_cbranch_scc1 .Lld%=\n\ts_add_u32 m0, %[mx], 41728\n\ts_nop 0\n\t" \
  "buffer_load_dwordx4 v48, %[rx], %0 offen offset:0 sc1 lds\n\tbuffer_load_dwordx4 v48, %[rx], %0 offen offset:1024 sc1 lds\n\t" \
  "buffer_load_dwordx4 v48, %[rx], %0 offen offset:2048 sc1 lds\n\tbuffer_load_dwordx4 v48, %[rx], %0 offen offset:3072 sc1 lds\n\ts_add_u32 %0, %0, 16384\n\t" \
  "s_bitset0_b32 %0, 18\n\ts_add_u32 m0, %[mb], 41728\n\ts_nop 0\n\tbuffer_load_dwordx4 v52, %[rw], %1 offen lds\n\ts_add_u32 m0, %[mb], 45952\n\ts_nop 0\n\t" \
  "buffer_load_dwordx4 v53, %[rw], %1 offen lds\n\ts_add_u32 m0, %[mb], 50176\n\ts_nop 0\n\tbuffer_load_dwordx4 v54, %[rw], %1 offen lds\n\ts_add_u32 m0, %[mb], 54400\n\ts_nop 0\n\t" \
  "buffer_load_dwordx4 v55, %[rw], %1 offen lds\n\ts_add_u32 m0, %[mb], 58624\n\ts_nop 0\n\tbuffer_load_dwordx4 v56, %[rw], %1 offen lds\n\ts_add_u32 m0, %[mb], 62848\n\ts_nop 0\n\t" \
  "buffer_load_dwordx4 v57, %[rw], %1 offen lds\n\ts_add_u32 %1, %1, 393216\n\ts_sub_u32 vcc_lo, %1, 6291456\n\ts_min_u32 %1, %1, vcc_lo\n\ts_waitcnt vmcnt(10)\n\ts_barrier\n\t" \
  "s_add_u32 m0, %[mx], 83456\n\ts_nop 0\n\tbuffer_load_dwordx4 v48, %[rx], %0 offen offset:0 sc1 lds\n\tbuffer_load_dwordx4 v48, %[rx], %0 offen offset:1024 sc1 lds\n\t" \
  "buffer_load_dwordx4 v48, %[rx], %0 offen offset:2048 sc1 lds\n\tbuffer_load_dwordx4 v48, %[rx], %0 offen offset:3072 sc1 lds\n\ts_add_u32 %0, %0, 16384\n\t" \
  "s_bitset0_b32 %0, 18\n\ts_add_u32 m0, %[mb], 83456\n\ts_nop 0\n\tbuffer_load_dwordx4 v52, %[rw], %1 offen lds\n\ts_add_u32 m0, %[mb], 87680\n\ts_nop 0\n\t" \
  "buffer_load_dwordx4 v53, %[rw], %1 offen lds\n\ts_add_u32 m0, %[mb], 91904\n\ts_nop 0\n\tbuffer_load_dwordx4 v54, %[rw], %1 offen lds\n\ts_add_u32 m0, %[mb], 96128\n\ts_nop 0\n\t" \
  "buffer_load_dwordx4 v55, %[rw], %1 offen lds\n\ts_add_u32 m0, %[mb], 100352\n\ts_nop 0\n\tbuffer_load_dwordx4 v56, %[rw], %1 offen lds\n\ts_add_u32 m0, %[mb], 104576\n\ts_nop 0\n\t" \
  "buffer_load_dwordx4 v57, %[rw], %1 offen lds\n\ts_add_u32 %1, %1, 393216\n\ts_sub_u32 vcc_lo, %1, 6291456\n\ts_min_u32 %1, %1, vcc_lo\n\ts_waitcnt vmcnt(10)\n\ts_barrier\n\t" \
  "s_add_u32 m0, %[mx], 0\n\ts_nop 0\n\tbuffer_load_dwordx4 v48, %[rx], %0 offen offset:0 sc1 lds\n\tbuffer_load_dwordx4 v48, %[rx], %0 offen offset:1024 sc1 lds\n\t" \
  "buffer_load_dwordx4 v48, %[rx], %0 offen offset:2048 sc1 lds\n\tbuffer_load_dwordx4 v48, %[rx], %0 offen offset:3072 sc1 lds\n\ts_add_u32 %0, %0, 16384\n\t" \
  "s_bitset0_b32 %0, 18\n\ts_add_u32 m0, %[mb], 0\n\ts_nop 0\n\tbuffer_load_dwordx4 v52, %[rw], %1 offen lds\n\ts_add_u32 m0, %[mb], 4224\n\ts_nop 0\n\t" \
  "buffer_load_dwordx4 v53, %[rw], %1 offen lds\n\ts_add_u32 m0, %[mb], 8448\n\ts_nop 0\n\tbuffer_load_dwordx4 v54, %[rw], %1 offen lds\n\ts_add_u32 m0, %[mb], 12672\n\ts_nop 0\n\t" \
  "buffer_load_dwordx4 v55, %[rw], %1 offen lds\n\ts_add_u32 m0, %[mb], 16896\n\ts_nop 0\n\tbuffer_load_dwordx4 v56, %[rw], %1 offen lds\n\ts_add_u32 m0, %[mb], 21120\n\ts_nop 0\n\t" \
  "buffer_load_dwordx4 v57, %[rw], %1 offen lds\n\ts_add_u32 %1, %1, 393216\n\ts_sub_u32 vcc_lo, %1, 6291456\n\ts_min_u32 %1, %1, vcc_lo\n\ts_waitcnt vmcnt(10)\n\ts_barrier\n\t"
#define LOAD_F0 \
  "s_nop 4\n\ts_waitcnt lgkmcnt(0)\n\ts_barrier\n\ts_barrier\n\ts_add_u32 m0, %[mb], 0\n\ts_nop 0\n\tbuffer_load_dwordx4 v52, %[rw], %1 offen lds\n\ts_add_u32 m0, %[mb], 4224\n\t" \
  "s_nop 0\n\tbuffer_load_dwordx4 v53, %[rw], %1 offen lds\n\ts_add_u32 m0, %[mb], 8448\n\ts_nop 0\n\tbuffer_load_dwordx4 v54, %[rw], %1 offen lds\n\ts_add_u32 m0, %[mb], 12672\n\t" \
  "s_nop 0\n\tbuffer_load_dwordx4 v55, %[rw], %1 offen lds\n\ts_add_u32 m0, %[mb], 16896\n\ts_nop 0\n\tbuffer_load_dwordx4 v56, %[rw], %1 offen lds\n\ts_add_u32 m0, %[mb], 21120\n\t" \
  "s_nop 0\n\tbuffer_load_dwordx4 v57, %[rw], %1 offen lds\n\ts_add_u32 m0, %[mb], 25344\n\ts_nop 0\n\tbuffer_load_dwordx4 v58, %[rw], %1 offen lds\n\ts_add_u32 %1, %1, 458752\n\t" \
  "s_sub_u32 vcc_lo, %1, 7340032\n\ts_min_u32 %1, %1, vcc_lo\n\ts_add_u32 m0, %[mb], 45952\n\ts_nop 0\n\tbuffer_load_dwordx4 v52, %[rw], %1 offen lds\n\t" \
  "s_add_u32 m0, %[mb], 50176\n\ts_nop 0\n\tbuffer_load_dwordx4 v53, %[rw], %1 offen lds\n\ts_add_u32 m0, %[mb], 54400\n\ts_nop 0\n\tbuffer_load_dwordx4 v54, %[rw], %1 offen lds\n\t" \
  "s_add_u32 m0, %[mb], 58624\n\ts_nop 0\n\tbuffer_load_dwordx4 v55, %[rw], %1 offen lds\n\ts_add_u32 m0, %[mb], 62848\n\ts_nop 0\n\tbuffer_load_dwordx4 v56, %[rw], %1 offen lds\n\t" \
  "s_add_u32 m0, %[mb], 67072\n\ts_nop 0\n\tbuffer_load_dwordx4 v57, %[rw], %1 offen lds\n\ts_add_u32 m0, %[mb], 71296\n\ts_nop 0\n\tbuffer_load_dwordx4 v58, %[rw], %1 offen lds\n\t" \
  "s_add_u32 %1, %1, 458752\n\ts_sub_u32 vcc_lo, %1, 7340032\n\ts_min_u32 %1, %1, vcc_lo\n\ts_barrier\n\ts_add_u32 m0, %[mb], 91904\n\ts_nop 0\n\t" \
  "buffer_load_dwordx4 v52, %[rw], %1 offen lds\n\ts_add_u32 m0, %[mb], 96128\n\ts_nop 0\n\tbuffer_load_dwordx4 v53, %[rw], %1 offen lds\n\ts_add_u32 m0, %[mb], 100352\n\ts_nop 0\n\t" \
  "buffer_load_dwordx4 v54, %[rw], %1 offen lds\n\ts_add_u32 m0, %[mb], 104576\n\ts_nop 0\n\tbuffer_load_dwordx4 v55, %[rw], %1 offen lds\n\ts_add_u32 m0, %[mb], 108800\n\ts_nop 0\n\t" \
  "buffer_load_dwordx4 v56, %[rw], %1 offen lds\n\ts_add_u32 m0, %[mb], 113024\n\ts_nop 0\n\tbuffer_load_dwordx4 v57, %[rw], %1 offen lds\n\ts_add_u32 m0, %[mb], 117248\n\ts_nop 0\n\t" \
  "buffer_load_dwordx4 v58, %[rw], %1 offen lds\n\ts_add_u32 %1, %1, 458752\n\ts_sub_u32 vcc_lo, %1, 7340032\n\ts_min_u32 %1, %1, vcc_lo\n\t"
#define LOAD_F \
  "s_add_u32 m0, %[mx], 0\n\ts_nop 0\n\tbuffer_load_dwordx4 v48, %[rx], %0 offen offset:0 sc1 lds\n\tbuffer_load_dwordx4 v48, %[rx], %0 offen offset:1024 sc1 lds\n\t" \
  "buffer_load_dwordx4 v48, %[rx], %0 offen offset:2048 sc1 lds\n\tbuffer_load_dwordx4 v48, %[rx], %0 offen offset:3072 sc1 lds\n\ts_add_u32 %0, %0, 16384\n\t" \
  "s_bitset0_b32 %0, 18\n\ts_add_u32 m0, %[mx], 45952\n\ts_nop 0\n\tbuffer_load_dwordx4 v48, %[rx], %0 offen offset:0 sc1 lds\n\t" \
  "buffer_load_dwordx4 v48, %[rx], %0 offen offset:1024 sc1 lds\n\tbuffer_load_dwordx4 v48, %[rx], %0 offen offset:2048 sc1 lds\n\t" \
  "buffer_load_dwordx4 v48, %[rx], %0 offen offset:3072 sc1 lds\n\ts_add_u32 %0, %0, 16384\n\ts_bitset0_b32 %0, 18\n\ts_add_u32 m0, %[mx], 91904\n\ts_nop 0\n\t" \
  "buffer_load_dwordx4 v48, %[rx], %0 offen offset:0 sc1 lds\n\tbuffer_load_dwordx4 v48, %[rx], %0 offen offset:1024 sc1 lds\n\t" \
  "buffer_load_dwordx4 v48, %[rx], %0 offen offset:2048 sc1 lds\n\tbuffer_load_dwordx4 v48, %[rx], %0 offen offset:3072 sc1 lds\n\ts_add_u32 %0, %0, 16384\n\t" \
  "s_bitset0_b32 %0, 18\n\ts_waitcnt vmcnt(8)\n\ts_barrier\n\tbuffer_load_dwordx4 v[0:3], v49, %[rs], 0 offen offset:0 sc1\n\t" \
  "buffer_load_dwordx4 v[4:7], v49, %[rs], 0 offen offset:512 sc1\n\tbuffer_load_dwordx4 v[8:11], v49, %[rs], 0 offen offset:1024 sc1\n\t" \
  "buffer_load_dwordx4 v[12:15], v49, %[rs], 0 offen offset:1536 sc1\n\ts_waitcnt vmcnt(8)\n\ts_barrier\n\ts_add_u32 m0, %[mx], 0\n\ts_nop 0\n\t" \
  "buffer_load_dwordx4 v48, %[rx], %0 offen offset:0 sc1 lds\n\tbuffer_load_dwordx4 v48, %[rx], %0 offen offset:1024 sc1 lds\n\t" \
  "buffer_load_dwordx4 v48, %[rx], %0 offen offset:2048 sc1 lds\n\tbuffer_load_dwordx4 v48, %[rx], %0 offen offset:3072 sc1 lds\n\ts_add_u32 %0, %0, 16384\n\t" \
  "s_bitset0_b32 %0, 18\n\ts_add_u32 m0, %[mb], 0\n\ts_nop 0\n\tbuffer_load_dwordx4 v52, %[rw], %1 offen lds\n\ts_add_u32 m0, %[mb], 4224\n\ts_nop 0\n\t" \
  "buffer_load_dwordx4 v53, %[rw], %1 offen lds\n\ts_add_u32 m0, %[mb], 8448\n\ts_nop 0\n\tbuffer_load_dwordx4 v54, %[rw], %1 offen lds\n\ts_add_u32 m0, %[mb], 12672\n\ts_nop 0\n\t" \
  "buffer_load_dwordx4 v55, %[rw], %1 offen lds\n\ts_add_u32 m0, %[mb], 16896\n\ts_nop 0\n\tbuffer_load_dwordx4 v56, %[rw], %1 offen lds\n\ts_add_u32 m0, %[mb], 21120\n\ts_nop 0\n\t" \
  "buffer_load_dwordx4 v57, %[rw], %1 offen lds\n\ts_add_u32 m0, %[mb], 25344\n\ts_nop 0\n\tbuffer_load_dwordx4 v58, %[rw], %1 offen lds\n\ts_add_u32 %1, %1, 458752\n\t" \
  "s_sub_u32 vcc_lo, %1, 7340032\n\ts_min_u32 %1, %1, vcc_lo\n\ts_waitcnt vmcnt(15)\n\ts_barrier\n\ts_mov_b32 %2, 3\n\t.Lld%=:\n\ts_add_u32 m0, %[mx], 45952\n\ts_nop 0\n\t" \
  "buffer_load_dwordx4 v48, %[rx], %0 offen offset:0 sc1 lds\n\tbuffer_load_dwordx4 v48, %[rx], %0 offen offset:1024 sc1 lds\n\t" \
  "buffer_load_dwordx4 v48, %[rx], %0 offen offset:2048 sc1 lds\n\tbuffer_load_dwordx4 v48, %[rx], %0 offen offset:3072 sc1 lds\n\ts_add_u32 %0, %0, 16384\n\t" \
  "s_bitset0_b32 %0, 18\n\ts_add_u32 m0, %[mb], 45952\n\ts_nop 0\n\tbuffer_load_dwordx4 v52, %[rw], %1 offen lds\n\ts_add_u32 m0, %[mb], 50176\n\ts_nop 0\n\t" \
  "buffer_load_dwordx4 v53, %[rw], %1 offen lds\n\ts_add_u32 m0, %[mb], 54400\n\ts_nop 0\n\tbuffer_load_dwordx4 v54, %[rw], %1 offen lds\n\ts_add_u32 m0, %[mb], 58624\n\ts_nop 0\n\t" \
  "buffer_load_dwordx4 v55, %[rw], %1 offen lds\n\ts_add_u32 m0, %[mb], 62848\n\ts_nop 0\n\tbuffer_load_dwordx4 v56, %[rw], %1 offen lds\n\ts_add_u32 m0, %[mb], 67072\n\ts_nop 0\n\t" \
  "buffer_load_dwordx4 v57, %[rw], %1 offen lds\n\ts_add_u32 m0, %[mb], 71296\n\ts_nop 0\n\tbuffer_load_dwordx4 v58, %[rw], %1 offen lds\n\ts_add_u32 %1, %1, 458752\n\t" \
  "s_sub_u32 vcc_lo, %1, 7340032\n\ts_min_u32 %1, %1, vcc_lo\n\ts_waitcnt vmcnt(11)\n\ts_barrier\n\ts_add_u32 m0, %[mx], 91904\n\ts_nop 0\n\t" \
  "buffer_load_dwordx4 v48, %[rx], %0 offen offset:0 sc1 lds\n\tbuffer_load_dwordx4 v48, %[rx], %0 offen offset:1024 sc1 lds\n\t" \
  "buffer_load_dwordx4 v48, %[rx], %0 offen offset:2048 sc1 lds\n\tbuffer_load_dwordx4 v48, %[rx], %0 offen offset:3072 sc1 lds\n\ts_add_u32 %0, %0, 16384\n\t" \
  "s_bitset0_b32 %0, 18\n\ts_add_u32 m0, %[mb], 91904\n\ts_nop 0\n\tbuffer_load_dwordx4 v52, %[rw], %1 offen lds\n\ts_add_u32 m0, %[mb], 96128\n\ts_nop 0\n\t" \
  "buffer_load_dwordx4 v53, %[rw], %1 offen lds\n\ts_add_u32 m0, %[mb], 100352\n\ts_nop 0\n\tbuffer_load_dwordx4 v54, %[rw], %1 offen lds\n\ts_add_u32 m0, %[mb], 104576\n\ts_nop 0\n\t" \
  "buffer_load_dwordx4 v55, %[rw], %1 offen lds\n\ts_add_u32 m0, %[mb], 108800\n\ts_nop 0\n\tbuffer_load_dwordx4 v56, %[rw], %1 offen lds\n\ts_add_u32 m0, %[mb], 113024\n\ts_nop 0\n\t" \
  "buffer_load_dwordx4 v57, %[rw], %1 offen lds\n\ts_add_u32 m0, %[mb], 117248\n\ts_nop 0\n\tbuffer_load_dwordx4 v58, %[rw], %1 offen lds\n\ts_add_u32 %1, %1, 458752\n\t" \
  "s_sub_u32 vcc_lo, %1, 7340032\n\ts_min_u32 %1, %1, vcc_lo\n\ts_waitcnt vmcnt(11)\n\ts_barrier\n\ts_add_u32 m0, %[mx], 0\n\ts_nop 0\n\t" \
  "buffer_load_dwordx4 v48, %[rx], %0 offen offset:0 sc1 lds\n\tbuffer_load_dwordx4 v48, %[rx], %0 offen offset:1024 sc1 lds\n\t" \
  "buffer_load_dwordx4 v48, %[rx], %0 offen offset:2048 sc1 lds\n\tbuffer_load_dwordx4 v48, %[rx], %0 offen offset:3072 sc1 lds\n\ts_add_u32 %0, %0, 16384\n\t" \
  "s_bitset0_b32 %0, 18\n\ts_add_u32 m0, %[mb], 0\n\ts_nop 0\n\tbuffer_load_dwordx4 v52, %[rw], %1 offen lds\n\ts_add_u32 m0, %[mb], 4224\n\ts_nop 0\n\t" \
  "buffer_load_dwordx4 v53, %[rw], %1 offen lds\n\ts_add_u32 m0, %[mb], 8448\n\ts_nop 0\n\tbuffer_load_dwordx4 v54, %[rw], %1 offen lds\n\ts_add_u32 m0, %[mb], 12672\n\ts_nop 0\n\t" \
  "buffer_load_dwordx4 v55, %[rw], %1 offen lds\n\ts_add_u32 m0, %[mb], 16896\n\ts_nop 0\n\tbuffer_load_dwordx4 v56, %[rw], %1 offen lds\n\ts_add_u32 m0, %[mb], 21120\n\ts_nop 0\n\t" \
  "buffer_load_dwordx4 v57, %[rw], %1 offen lds\n\ts_add_u32 m0, %[mb], 25344\n\ts_nop 0\n\tbuffer_load_dwordx4 v58, %[rw], %1 offen lds\n\ts_add_u32 %1, %1, 458752\n\t" \
  "s_sub_u32 vcc_lo, %1, 7340032\n\ts_min_u32 %1, %1, vcc_lo\n\ts_sub_u32 %2, %2, 1\n\ts_cmp_lg_u32 %2, 0\n\ts_waitcnt vmcnt(11)\n\ts_barrier\n\ts_cbranch_scc1 .Lld%=\n\t" \
  "s_add_u32 m0, %[mx], 45952\n\ts_nop 0\n\tbuffer_load_dwordx4 v48, %[rx], %0 offen offset:0 sc1 lds\n\tbuffer_load_dwordx4 v48, %[rx], %0 offen offset:1024 sc1 lds\n\t" \
  "buffer_load_dwordx4 v48, %[rx], %0 offen offset:2048 sc1 lds\n\tbuffer_load_dwordx4 v48, %[rx], %0 offen offset:3072 sc1 lds\n\ts_add_u32 %0, %0, 16384\n\t" \
  "s_bitset0_b32 %0, 18\n\ts_add_u32 m0, %[mb], 45952\n\ts_nop 0\n\tbuffer_load_dwordx4 v52, %[rw], %1 offen lds\n\ts_add_u32 m0, %[mb], 50176\n\ts_nop 0\n\t" \
  "buffer_load_dwordx4 v53, %[rw], %1 offen lds\n\ts_add_u32 m0, %[mb], 54400\n\ts_nop 0\n\tbuffer_load_dwordx4 v54, %[rw], %1 offen lds\n\ts_add_u32 m0, %[mb], 58624\n\ts_nop 0\n\t" \
  "buffer_load_dwordx4 v55, %[rw], %1 offen lds\n\ts_add_u32 m0, %[mb], 62848\n\ts_nop 0\n\tbuffer_load_dwordx4 v56, %[rw], %1 offen lds\n\ts_add_u32 m0, %[mb], 67072\n\ts_nop 0\n\t" \
  "buffer_load_dwordx4 v57, %[rw], %1 offen lds\n\ts_add_u32 m0, %[mb], 71296\n\ts_nop 0\n\tbuffer_load_dwordx4 v58, %[rw], %1 offen lds\n\ts_add_u32 %1, %1, 458752\n\t" \
  "s_sub_u32 vcc_lo, %1, 7340032\n\ts_min_u32 %1, %1, vcc_lo\n\ts_waitcnt vmcnt(11)\n\ts_barrier\n\ts_add_u32 m0, %[mx], 91904\n\ts_nop 0\n\t" \
  "buffer_load_dwordx4 v48, %[rx], %0 offen offset:0 sc1 lds\n\tbuffer_load_dwordx4 v48, %[rx], %0 offen offset:1024 sc1 lds\n\t" \
  "buffer_load_dwordx4 v48, %[rx], %0 offen offset:2048 sc1 lds\n\tbuffer_load_dwordx4 v48, %[rx], %0 offen offset:3072 sc1 lds\n\ts_add_u32 %0, %0, 16384\n\t" \
  "s_bitset0_b32 %0, 18\n\ts_add_u32 m0, %[mb], 91904\n\ts_nop 0\n\tbuffer_load_dwordx4 v52, %[rw], %1 offen lds\n\ts_add_u32 m0, %[mb], 96128\n\ts_nop 0\n\t" \
  "buffer_load_dwordx4 v53, %[rw], %1 offen lds\n\ts_add_u32 m0, %[mb], 100352\n\ts_nop 0\n\tbuffer_load_dwordx4 v54, %[rw], %1 offen lds\n\ts_add_u32 m0, %[mb], 104576\n\ts_nop 0\n\t" \
  "buffer_load_dwordx4 v55, %[rw], %1 offen lds\n\ts_add_u32 m0, %[mb], 108800\n\ts_nop 0\n\tbuffer_load_dwordx4 v56, %[rw], %1 offen lds\n\ts_add_u32 m0, %[mb], 113024\n\ts_nop 0\n\t" \
  "buffer_load_dwordx4 v57, %[rw], %1 offen lds\n\ts_add_u32 m0, %[mb], 117248\n\ts_nop 0\n\tbuffer_load_dwordx4 v58, %[rw], %1 offen lds\n\ts_add_u32 %1, %1, 458752\n\t" \
  "s_sub_u32 vcc_lo, %1, 7340032\n\ts_min_u32 %1, %1, vcc_lo\n\ts_waitcnt vmcnt(11)\n\ts_barrier\n\ts_add_u32 m0, %[mx], 0\n\ts_nop 0\n\t" \
  "buffer_load_dwordx4 v48, %[rx], %0 offen offset:0 sc1 lds\n\tbuffer_load_dwordx4 v48, %[rx], %0 offen offset:1024 sc1 lds\n\t" \
  "buffer_load_dwordx4 v48, %[rx], %0 offen offset:2048 sc1 lds\n\tbuffer_load_dwordx4 v48, %[rx], %0 offen offset:3072 sc1 lds\n\ts_add_u32 %0, %0, 16384\n\t" \
  "s_bitset0_b32 %0, 18\n\ts_add_u32 m0, %[mb], 0\n\ts_nop 0\n\tbuffer_load_dwordx4 v52, %[rw], %1 offen lds\n\ts_add_u32 m0, %[mb], 4224\n\ts_nop 0\n\t" \
  "buffer_load_dwordx4 v53, %[rw], %1 offen lds\n\ts_add_u32 m0, %[mb], 8448\n\ts_nop 0\n\tbuffer_load_dwordx4 v54, %[rw], %1 offen lds\n\ts_add_u32 m0, %[mb], 12672\n\ts_nop 0\n\t" \
  "buffer_load_dwordx4 v55, %[rw], %1 offen lds\n\ts_add_u32 m0, %[mb], 16896\n\ts_nop 0\n\tbuffer_load_dwordx4 v56, %[rw], %1 offen lds\n\ts_add_u32 m0, %[mb], 21120\n\ts_nop 0\n\t" \
  "buffer_load_dwordx4 v57, %[rw], %1 offen lds\n\ts_add_u32 m0, %[mb], 25344\n\ts_nop 0\n\tbuffer_load_dwordx4 v58, %[rw], %1 offen lds\n\ts_add_u32 %1, %1, 458752\n\t" \
  "s_sub_u32 vcc_lo, %1, 7340032\n\ts_min_u32 %1, %1, vcc_lo\n\ts_waitcnt vmcnt(11)\n\ts_barrier\n\t"
#define LOAD_CLOB \
  "v64", "v65", "v66", "v67", "v68", "v69", "v70", "v71", "v72", "v73", "v74", "v75", "v76", "v77", "v78", "v79", "v80", "v81", "v82", "v83", "v84", "v85", "v86", "v87", \
  "v88", "v89", "v90", "v91", "v92", "v93", "v94", "v95", "v96", "v97", "v98", "v99", "v100", "v101", "v102", "v103", "v104", "v105", "v106", "v107", "v108", "v109", \
  "v110", "v111"
#define FFN2_LOOP \
  "s_waitcnt lgkmcnt(0)\n\ts_barrier\n\tds_read_b128 v[64:67], v144 offset:0\n\tds_read_b128 v[80:83], v145 offset:0\n\tds_read_b128 v[68:71], v144 offset:2048\n\t" \
  "ds_read_b128 v[72:75], v144 offset:4096\n\tds_read_b128 v[76:79], v144 offset:6144\n\tds_read_b128 v[84:87], v145 offset:1024\n\t" \
  "ds_read_b128 v[88:91], v145 offset:8192\n\tds_read_b128 v[92:95], v145 offset:9216\n\ts_mov_b32 %2, 6\n\ts_waitcnt vmcnt(7)\n\t" \
  "buffer_store_dwordx2 v[150:151], v148, %0, 0 offen sc1\n\ts_waitcnt lgkmcnt(3)\n\tv_mfma_f32_16x16x32_bf16 v[4:7], v[80:83], v[68:71], v[0:3]\n\t" \
  "ds_read_b128 v[96:99], v144 offset:1024\n\tv_mfma_f32_16x16x32_bf16 v[8:11], v[80:83], v[72:75], v[0:3]\n\tds_read_b128 v[112:115], v145 offset:4096\n\t" \
  "v_mfma_f32_16x16x32_bf16 v[12:15], v[80:83], v[76:79], v[0:3]\n\tds_read_b128 v[100:103], v144 offset:3072\n\t" \
  "v_mfma_f32_16x16x32_bf16 v[0:3], v[80:83], v[64:67], v[0:3]\n\ts_waitcnt lgkmcnt(5)\n\tv_mfma_f32_16x16x32_bf16 v[20:23], v[84:87], v[68:71], v[16:19]\n\t" \
  "ds_read_b128 v[104:107], v144 offset:5120\n\tv_mfma_f32_16x16x32_bf16 v[24:27], v[84:87], v[72:75], v[16:19]\n\tds_read_b128 v[108:111], v144 offset:7168\n\t" \
  "v_mfma_f32_16x16x32_bf16 v[28:31], v[84:87], v[76:79], v[16:19]\n\tds_read_b128 v[116:119], v145 offset:5120\n\t" \
  "v_mfma_f32_16x16x32_bf16 v[16:19], v[84:87], v[64:67], v[16:19]\n\ts_waitcnt lgkmcnt(6)\n\tv_mfma_f32_16x16x32_bf16 v[36:39], v[88:91], v[68:71], 0\n\t" \
  "ds_read_b128 v[120:123], v145 offset:12288\n\tv_mfma_f32_16x16x32_bf16 v[40:43], v[88:91], v[72:75], 0\n\tds_read_b128 v[124:127], v145 offset:13312\n\t" \
  "v_mfma_f32_16x16x32_bf16 v[44:47], v[88:91], v[76:79], 0\n\tv_mfma_f32_16x16x32_bf16 v[32:35], v[88:91], v[64:67], 0\n\t" \
  "v_mfma_f32_16x16x32_bf16 v[52:55], v[92:95], v[68:71], 0\n\tv_mfma_f32_16x16x32_bf16 v[56:59], v[92:95], v[72:75], 0\n\t" \
  "v_mfma_f32_16x16x32_bf16 v[60:63], v[92:95], v[76:79], 0\n\tv_mfma_f32_16x16x32_bf16 v[48:51], v[92:95], v[64:67], 0\n\ts_branch .Lf2m%=\n\t.p2align 6\n\t.Lf2l%=:\n\t" \
  "s_waitcnt lgkmcnt(3)\n\tv_mfma_f32_16x16x32_bf16 v[0:3], v[80:83], v[64:67], v[0:3]\n\tds_read_b128 v[96:99], v144 offset:1024\n\t" \
  "v_mfma_f32_16x16x32_bf16 v[4:7], v[80:83], v[68:71], v[4:7]\n\tds_read_b128 v[112:115], v145 offset:4096\n\t" \
  "v_mfma_f32_16x16x32_bf16 v[8:11], v[80:83], v[72:75], v[8:11]\n\tds_read_b128 v[100:103], v144 offset:3072\n\t" \
  "v_mfma_f32_16x16x32_bf16 v[12:15], v[80:83], v[76:79], v[12:15]\n\ts_waitcnt lgkmcnt(5)\n\tv_mfma_f32_16x16x32_bf16 v[16:19], v[84:87], v[64:67], v[16:19]\n\t" \
  "ds_read_b128 v[104:107], v144 offset:5120\n\tv_mfma_f32_16x16x32_bf16 v[20:23], v[84:87], v[68:71], v[20:23]\n\tds_read_b128 v[108:111], v144 offset:7168\n\t" \
  "v_mfma_f32_16x16x32_bf16 v[24:27], v[84:87], v[72:75], v[24:27]\n\tds_read_b128 v[116:119], v145 offset:5120\n\t" \
  "v_mfma_f32_16x16x32_bf16 v[28:31], v[84:87], v[76:79], v[28:31]\n\ts_waitcnt lgkmcnt(6)\n\tv_mfma_f32_16x16x32_bf16 v[32:35], v[88:91], v[64:67], v[32:35]\n\t" \
  "ds_read_b128 v[120:123], v145 offset:12288\n\tv_mfma_f32_16x16x32_bf16 v[36:39], v[88:91], v[68:71], v[36:39]\n\tds_read_b128 v[124:127], v145 offset:13312\n\t" \
  "v_mfma_f32_16x16x32_bf16 v[40:43], v[88:91], v[72:75], v[40:43]\n\tv_mfma_f32_16x16x32_bf16 v[44:47], v[88:91], v[76:79], v[44:47]\n\t" \
  "v_mfma_f32_16x16x32_bf16 v[48:51], v[92:95], v[64:67], v[48:51]\n\tv_mfma_f32_16x16x32_bf16 v[52:55], v[92:95], v[68:71], v[52:55]\n\t" \
  "v_mfma_f32_16x16x32_bf16 v[56:59], v[92:95], v[72:75], v[56:59]\n\tv_mfma_f32_16x16x32_bf16 v[60:63], v[92:95], v[76:79], v[60:63]\n\t.Lf2m%=:\n\ts_waitcnt lgkmcnt(3)\n\t" \
  "v_mfma_f32_16x16x32_bf16 v[0:3], v[112:115], v[96:99], v[0:3]\n\tv_mov_b32 v187, v150\n\tv_mfma_f32_16x16x32_bf16 v[4:7], v[112:115], v[100:103], v[4:7]\n\t" \
  "v_mov_b32 v191, v150\n\tv_mfma_f32_16x16x32_bf16 v[8:11], v[112:115], v[104:107], v[8:11]\n\tv_mov_b32 v195, v150\n\t" \
  "v_mfma_f32_16x16x32_bf16 v[12:15], v[112:115], v[108:111], v[12:15]\n\ts_waitcnt lgkmcnt(0)\n\ts_barrier\n\t" \
  "v_mfma_f32_16x16x32_bf16 v[16:19], v[116:119], v[96:99], v[16:19]\n\tds_read_b128 v[64:67], v144 offset:16384\n\t" \
  "v_mfma_f32_16x16x32_bf16 v[20:23], v[116:119], v[100:103], v[20:23]\n\tds_read_b128 v[80:83], v145 offset:16384\n\t" \
  "v_mfma_f32_16x16x32_bf16 v[24:27], v[116:119], v[104:107], v[24:27]\n\tds_read_b128 v[68:71], v144 offset:18432\n\t" \
  "v_mfma_f32_16x16x32_bf16 v[28:31], v[116:119], v[108:111], v[28:31]\n\tds_read_b128 v[72:75], v144 offset:20480\n\t" \
  "v_mfma_f32_16x16x32_bf16 v[32:35], v[120:123], v[96:99], v[32:35]\n\tds_read_b128 v[76:79], v144 offset:22528\n\t" \
  "v_mfma_f32_16x16x32_bf16 v[36:39], v[120:123], v[100:103], v[36:39]\n\tds_read_b128 v[84:87], v145 offset:17408\n\t" \
  "v_mfma_f32_16x16x32_bf16 v[40:43], v[120:123], v[104:107], v[40:43]\n\tds_read_b128 v[88:91], v145 offset:24576\n\t" \
  "v_mfma_f32_16x16x32_bf16 v[44:47], v[120:123], v[108:111], v[44:47]\n\tds_read_b128 v[92:95], v145 offset:25600\n\t" \
  "v_mfma_f32_16x16x32_bf16 v[48:51], v[124:127], v[96:99], v[48:51]\n\tv_mfma_f32_16x16x32_bf16 v[52:55], v[124:127], v[100:103], v[52:55]\n\t" \
  "v_mfma_f32_16x16x32_bf16 v[56:59], v[124:127], v[104:107], v[56:59]\n\tv_mfma_f32_16x16x32_bf16 v[60:63], v[124:127], v[108:111], v[60:63]\n\ts_waitcnt lgkmcnt(3)\n\t" \
  "v_mfma_f32_16x16x32_bf16 v[0:3], v[80:83], v[64:67], v[0:3]\n\tds_read_b128 v[96:99], v144 offset:17408\n\tv_mfma_f32_16x16x32_bf16 v[4:7], v[80:83], v[68:71], v[4:7]\n\t" \
  "ds_read_b128 v[112:115], v145 offset:20480\n\tv_mfma_f32_16x16x32_bf16 v[8:11], v[80:83], v[72:75], v[8:11]\n\tds_read_b128 v[100:103], v144 offset:19456\n\t" \
  "v_mfma_f32_16x16x32_bf16 v[12:15], v[80:83], v[76:79], v[12:15]\n\ts_waitcnt lgkmcnt(5)\n\tv_mfma_f32_16x16x32_bf16 v[16:19], v[84:87], v[64:67], v[16:19]\n\t" \
  "ds_read_b128 v[104:107], v144 offset:21504\n\tv_mfma_f32_16x16x32_bf16 v[20:23], v[84:87], v[68:71], v[20:23]\n\tds_read_b128 v[108:111], v144 offset:23552\n\t" \
  "v_mfma_f32_16x16x32_bf16 v[24:27], v[84:87], v[72:75], v[24:27]\n\tds_read_b128 v[116:119], v145 offset:21504\n\t" \
  "v_mfma_f32_16x16x32_bf16 v[28:31], v[84:87], v[76:79], v[28:31]\n\ts_waitcnt lgkmcnt(6)\n\tv_mfma_f32_16x16x32_bf16 v[32:35], v[88:91], v[64:67], v[32:35]\n\t" \
  "ds_read_b128 v[120:123], v145 offset:28672\n\tv_mfma_f32_16x16x32_bf16 v[36:39], v[88:91], v[68:71], v[36:39]\n\tds_read_b128 v[124:127], v145 offset:29696\n\t" \
  "v_mfma_f32_16x16x32_bf16 v[40:43], v[88:91], v[72:75], v[40:43]\n\tv_mfma_f32_16x16x32_bf16 v[44:47], v[88:91], v[76:79], v[44:47]\n\t" \
  "v_mfma_f32_16x16x32_bf16 v[48:51], v[92:95], v[64:67], v[48:51]\n\tv_mfma_f32_16x16x32_bf16 v[52:55], v[92:95], v[68:71], v[52:55]\n\t" \
  "v_mfma_f32_16x16x32_bf16 v[56:59], v[92:95], v[72:75], v[56:59]\n\tv_mfma_f32_16x16x32_bf16 v[60:63], v[92:95], v[76:79], v[60:63]\n\ts_waitcnt lgkmcnt(3)\n\t" \
  "v_mfma_f32_16x16x32_bf16 v[0:3], v[112:115], v[96:99], v[0:3]\n\tv_mov_b32 v199, v150\n\tv_mfma_f32_16x16x32_bf16 v[4:7], v[112:115], v[100:103], v[4:7]\n\t" \
  "v_mov_b32 v203, v150\n\tv_mfma_f32_16x16x32_bf16 v[8:11], v[112:115], v[104:107], v[8:11]\n\tv_mov_b32 v207, v150\n\t" \
  "v_mfma_f32_16x16x32_bf16 v[12:15], v[112:115], v[108:111], v[12:15]\n\ts_waitcnt lgkmcnt(0)\n\ts_barrier\n\t" \
  "v_mfma_f32_16x16x32_bf16 v[16:19], v[116:119], v[96:99], v[16:19]\n\tds_read_b128 v[64:67], v144 offset:32768\n\t" \
  "v_mfma_f32_16x16x32_bf16 v[20:23], v[116:119], v[100:103], v[20:23]\n\tds_read_b128 v[80:83], v145 offset:32768\n\t" \
  "v_mfma_f32_16x16x32_bf16 v[24:27], v[116:119], v[104:107], v[24:27]\n\tds_read_b128 v[68:71], v144 offset:34816\n\t" \
  "v_mfma_f32_16x16x32_bf16 v[28:31], v[116:119], v[108:111], v[28:31]\n\tds_read_b128 v[72:75], v144 offset:36864\n\t" \
  "v_mfma_f32_16x16x32_bf16 v[32:35], v[120:123], v[96:99], v[32:35]\n\tds_read_b128 v[76:79], v144 offset:38912\n\t" \
  "v_mfma_f32_16x16x32_bf16 v[36:39], v[120:123], v[100:103], v[36:39]\n\tds_read_b128 v[84:87], v145 offset:33792\n\t" \
  "v_mfma_f32_16x16x32_bf16 v[40:43], v[120:123], v[104:107], v[40:43]\n\tds_read_b128 v[88:91], v145 offset:40960\n\t" \
  "v_mfma_f32_16x16x32_bf16 v[44:47], v[120:123], v[108:111], v[44:47]\n\tds_read_b128 v[92:95], v145 offset:41984\n\t" \
  "v_mfma_f32_16x16x32_bf16 v[48:51], v[124:127], v[96:99], v[48:51]\n\tv_mfma_f32_16x16x32_bf16 v[52:55], v[124:127], v[100:103], v[52:55]\n\t" \
  "v_mfma_f32_16x16x32_bf16 v[56:59], v[124:127], v[104:107], v[56:59]\n\tv_mfma_f32_16x16x32_bf16 v[60:63], v[124:127], v[108:111], v[60:63]\n\ts_waitcnt lgkmcnt(3)\n\t" \
  "v_mfma_f32_16x16x32_bf16 v[0:3], v[80:83], v[64:67], v[0:3]\n\tds_read_b128 v[96:99], v144 offset:33792\n\tv_mfma_f32_16x16x32_bf16 v[4:7], v[80:83], v[68:71], v[4:7]\n\t" \
  "ds_read_b128 v[112:115], v145 offset:36864\n\tv_mfma_f32_16x16x32_bf16 v[8:11], v[80:83], v[72:75], v[8:11]\n\tds_read_b128 v[100:103], v144 offset:35840\n\t" \
  "v_mfma_f32_16x16x32_bf16 v[12:15], v[80:83], v[76:79], v[12:15]\n\ts_waitcnt lgkmcnt(5)\n\tv_mfma_f32_16x16x32_bf16 v[16:19], v[84:87], v[64:67], v[16:19]\n\t" \
  "ds_read_b128 v[104:107], v144 offset:37888\n\tv_mfma_f32_16x16x32_bf16 v[20:23], v[84:87], v[68:71], v[20:23]\n\tds_read_b128 v[108:111], v144 offset:39936\n\t" \
  "v_mfma_f32_16x16x32_bf16 v[24:27], v[84:87], v[72:75], v[24:27]\n\tds_read_b128 v[116:119], v145 offset:37888\n\t" \
  "v_mfma_f32_16x16x32_bf16 v[28:31], v[84:87], v[76:79], v[28:31]\n\ts_waitcnt lgkmcnt(6)\n\tv_mfma_f32_16x16x32_bf16 v[32:35], v[88:91], v[64:67], v[32:35]\n\t" \
  "ds_read_b128 v[120:123], v145 offset:45056\n\tv_mfma_f32_16x16x32_bf16 v[36:39], v[88:91], v[68:71], v[36:39]\n\tds_read_b128 v[124:127], v145 offset:46080\n\t" \
  "v_mfma_f32_16x16x32_bf16 v[40:43], v[88:91], v[72:75], v[40:43]\n\tv_mfma_f32_16x16x32_bf16 v[44:47], v[88:91], v[76:79], v[44:47]\n\t" \
  "v_mfma_f32_16x16x32_bf16 v[48:51], v[92:95], v[64:67], v[48:51]\n\tv_mfma_f32_16x16x32_bf16 v[52:55], v[92:95], v[68:71], v[52:55]\n\t" \
  "v_mfma_f32_16x16x32_bf16 v[56:59], v[92:95], v[72:75], v[56:59]\n\tv_mfma_f32_16x16x32_bf16 v[60:63], v[92:95], v[76:79], v[60:63]\n\ts_waitcnt lgkmcnt(3)\n\t" \
  "v_mfma_f32_16x16x32_bf16 v[0:3], v[112:115], v[96:99], v[0:3]\n\tv_mfma_f32_16x16x32_bf16 v[4:7], v[112:115], v[100:103], v[4:7]\n\t" \
  "v_mfma_f32_16x16x32_bf16 v[8:11], v[112:115], v[104:107], v[8:11]\n\ts_waitcnt vmcnt(0)\n\tv_mfma_f32_16x16x32_bf16 v[12:15], v[112:115], v[108:111], v[12:15]\n\t" \
  "s_waitcnt lgkmcnt(0)\n\ts_barrier\n\tv_mfma_f32_16x16x32_bf16 v[16:19], v[116:119], v[96:99], v[16:19]\n\tds_read_b128 v[64:67], v144 offset:49152\n\t" \
  "v_mfma_f32_16x16x32_bf16 v[20:23], v[116:119], v[100:103], v[20:23]\n\tds_read_b128 v[80:83], v145 offset:49152\n\t" \
  "v_mfma_f32_16x16x32_bf16 v[24:27], v[116:119], v[104:107], v[24:27]\n\tds_read_b128 v[68:71], v144 offset:51200\n\t" \
  "v_mfma_f32_16x16x32_bf16 v[28:31], v[116:119], v[108:111], v[28:31]\n\tds_read_b128 v[72:75], v144 offset:53248\n\t" \
  "v_mfma_f32_16x16x32_bf16 v[32:35], v[120:123], v[96:99], v[32:35]\n\tds_read_b128 v[76:79], v144 offset:55296\n\t" \
  "v_mfma_f32_16x16x32_bf16 v[36:39], v[120:123], v[100:103], v[36:39]\n\tds_read_b128 v[84:87], v145 offset:50176\n\t" \
  "v_mfma_f32_16x16x32_bf16 v[40:43], v[120:123], v[104:107], v[40:43]\n\tds_read_b128 v[88:91], v145 offset:57344\n\t" \
  "v_mfma_f32_16x16x32_bf16 v[44:47], v[120:123], v[108:111], v[44:47]\n\tds_read_b128 v[92:95], v145 offset:58368\n\t" \
  "v_mfma_f32_16x16x32_bf16 v[48:51], v[124:127], v[96:99], v[48:51]\n\tv_mfma_f32_16x16x32_bf16 v[52:55], v[124:127], v[100:103], v[52:55]\n\t" \
  "v_mfma_f32_16x16x32_bf16 v[56:59], v[124:127], v[104:107], v[56:59]\n\tv_mfma_f32_16x16x32_bf16 v[60:63], v[124:127], v[108:111], v[60:63]\n\ts_waitcnt lgkmcnt(3)\n\t" \
  "v_mfma_f32_16x16x32_bf16 v[0:3], v[80:83], v[64:67], v[0:3]\n\tds_read_b128 v[96:99], v144 offset:50176\n\tv_mfma_f32_16x16x32_bf16 v[4:7], v[80:83], v[68:71], v[4:7]\n\t" \
  "ds_read_b128 v[112:115], v145 offset:53248\n\tv_mfma_f32_16x16x32_bf16 v[8:11], v[80:83], v[72:75], v[8:11]\n\tds_read_b128 v[100:103], v144 offset:52224\n\t" \
  "v_mfma_f32_16x16x32_bf16 v[12:15], v[80:83], v[76:79], v[12:15]\n\ts_waitcnt lgkmcnt(5)\n\tv_mfma_f32_16x16x32_bf16 v[16:19], v[84:87], v[64:67], v[16:19]\n\t" \
  "ds_read_b128 v[104:107], v144 offset:54272\n\tv_mfma_f32_16x16x32_bf16 v[20:23], v[84:87], v[68:71], v[20:23]\n\tds_read_b128 v[108:111], v144 offset:56320\n\t" \
  "v_mfma_f32_16x16x32_bf16 v[24:27], v[84:87], v[72:75], v[24:27]\n\tds_read_b128 v[116:119], v145 offset:54272\n\t" \
  "v_mfma_f32_16x16x32_bf16 v[28:31], v[84:87], v[76:79], v[28:31]\n\ts_waitcnt lgkmcnt(6)\n\tv_mfma_f32_16x16x32_bf16 v[32:35], v[88:91], v[64:67], v[32:35]\n\t" \
  "ds_read_b128 v[120:123], v145 offset:61440\n\tv_mfma_f32_16x16x32_bf16 v[36:39], v[88:91], v[68:71], v[36:39]\n\tds_read_b128 v[124:127], v145 offset:62464\n\t" \
  "v_mfma_f32_16x16x32_bf16 v[40:43], v[88:91], v[72:75], v[40:43]\n\tv_mfma_f32_16x16x32_bf16 v[44:47], v[88:91], v[76:79], v[44:47]\n\t" \
  "v_mfma_f32_16x16x32_bf16 v[48:51], v[92:95], v[64:67], v[48:51]\n\tv_mfma_f32_16x16x32_bf16 v[52:55], v[92:95], v[68:71], v[52:55]\n\t" \
  "v_mfma_f32_16x16x32_bf16 v[56:59], v[92:95], v[72:75], v[56:59]\n\tv_mfma_f32_16x16x32_bf16 v[60:63], v[92:95], v[76:79], v[60:63]\n\ts_waitcnt lgkmcnt(3)\n\t" \
  "v_mfma_f32_16x16x32_bf16 v[0:3], v[112:115], v[96:99], v[0:3]\n\ts_add_u32 %3, %1, 0x1000\n\tv_mfma_f32_16x16x32_bf16 v[4:7], v[112:115], v[100:103], v[4:7]\n\t" \
  "s_sub_u32 %2, %2, 1\n\tv_mfma_f32_16x16x32_bf16 v[8:11], v[112:115], v[104:107], v[8:11]\n\ts_cmp_lg_u32 %2, 0\n\t" \
  "v_mfma_f32_16x16x32_bf16 v[12:15], v[112:115], v[108:111], v[12:15]\n\ts_waitcnt lgkmcnt(0)\n\ts_barrier\n\t" \
  "v_mfma_f32_16x16x32_bf16 v[16:19], v[116:119], v[96:99], v[16:19]\n\tds_read_b128 v[64:67], v144 offset:0\n\t" \
  "v_mfma_f32_16x16x32_bf16 v[20:23], v[116:119], v[100:103], v[20:23]\n\tds_read_b128 v[80:83], v145 offset:0\n\t" \
  "v_mfma_f32_16x16x32_bf16 v[24:27], v[116:119], v[104:107], v[24:27]\n\tds_read_b128 v[68:71], v144 offset:2048\n\t" \
  "v_mfma_f32_16x16x32_bf16 v[28:31], v[116:119], v[108:111], v[28:31]\n\tds_read_b128 v[72:75], v144 offset:4096\n\t" \
  "v_mfma_f32_16x16x32_bf16 v[32:35], v[120:123], v[96:99], v[32:35]\n\tds_read_b128 v[76:79], v144 offset:6144\n\t" \
  "v_mfma_f32_16x16x32_bf16 v[36:39], v[120:123], v[100:103], v[36:39]\n\tds_read_b128 v[84:87], v145 offset:1024\n\t" \
  "v_mfma_f32_16x16x32_bf16 v[40:43], v[120:123], v[104:107], v[40:43]\n\tds_read_b128 v[88:91], v145 offset:8192\n\t" \
  "v_mfma_f32_16x16x32_bf16 v[44:47], v[120:123], v[108:111], v[44:47]\n\tds_read_b128 v[92:95], v145 offset:9216\n\t" \
  "v_mfma_f32_16x16x32_bf16 v[48:51], v[124:127], v[96:99], v[48:51]\n\tv_mfma_f32_16x16x32_bf16 v[52:55], v[124:127], v[100:103], v[52:55]\n\t" \
  "v_mfma_f32_16x16x32_bf16 v[56:59], v[124:127], v[104:107], v[56:59]\n\tv_mfma_f32_16x16x32_bf16 v[60:63], v[124:127], v[108:111], v[60:63]\n\ts_cbranch_scc1 .Lf2l%=\n\t" \
  "s_waitcnt lgkmcnt(3)\n\tv_mfma_f32_16x16x32_bf16 v[0:3], v[80:83], v[64:67], v[0:3]\n\tds_read_b128 v[96:99], v144 offset:1024\n\t" \
  "ds_read_b128 v[112:115], v145 offset:4096\n\tv_cvt_pk_bf16_f32 v184, v32, v33\n\tv_cvt_pk_bf16_f32 v185, v34, v35\n\tv_cvt_pk_bf16_f32 v186, v36, v37\n\t" \
  "v_mfma_f32_16x16x32_bf16 v[4:7], v[80:83], v[68:71], v[4:7]\n\tds_read_b128 v[100:103], v144 offset:3072\n\tds_read_b128 v[104:107], v144 offset:5120\n\t" \
  "buffer_store_dwordx4 v[184:187], v146, %0, %1 offen offset:0 sc1\n\tv_mfma_f32_16x16x32_bf16 v[8:11], v[80:83], v[72:75], v[8:11]\n\t" \
  "ds_read_b128 v[108:111], v144 offset:7168\n\tds_read_b128 v[116:119], v145 offset:5120\n\tv_cvt_pk_bf16_f32 v188, v38, v39\n\tv_cvt_pk_bf16_f32 v189, v40, v41\n\t" \
  "v_cvt_pk_bf16_f32 v190, v42, v43\n\tv_mfma_f32_16x16x32_bf16 v[12:15], v[80:83], v[76:79], v[12:15]\n\t" \
  "buffer_store_dwordx4 v[188:191], v146, %0, %1 offen offset:1024 sc1\n\ts_waitcnt lgkmcnt(8)\n\tv_mfma_f32_16x16x32_bf16 v[16:19], v[84:87], v[64:67], v[16:19]\n\t" \
  "s_barrier\n\tds_read_b128 v[160:163], v144 offset:16384\n\tv_cvt_pk_bf16_f32 v192, v44, v45\n\tv_cvt_pk_bf16_f32 v193, v46, v47\n\tv_cvt_pk_bf16_f32 v194, v48, v49\n\t" \
  "v_mfma_f32_16x16x32_bf16 v[20:23], v[84:87], v[68:71], v[20:23]\n\tds_read_b128 v[176:179], v145 offset:8192\n\t" \
  "buffer_store_dwordx4 v[192:195], v146, %0, %1 offen offset:2048 sc1\n\tv_mfma_f32_16x16x32_bf16 v[24:27], v[84:87], v[72:75], v[24:27]\n\t" \
  "ds_read_b128 v[164:167], v144 offset:18432\n\tv_cvt_pk_bf16_f32 v196, v50, v51\n\tv_cvt_pk_bf16_f32 v197, v52, v53\n\tv_cvt_pk_bf16_f32 v198, v54, v55\n\t" \
  "v_mfma_f32_16x16x32_bf16 v[28:31], v[84:87], v[76:79], v[28:31]\n\tds_read_b128 v[168:171], v144 offset:20480\n\t" \
  "buffer_store_dwordx4 v[196:199], v146, %0, %1 offen offset:3072 sc1\n\ts_waitcnt lgkmcnt(5)\n\tv_mfma_f32_16x16x32_bf16 v[0:3], v[112:115], v[96:99], v[0:3]\n\t" \
  "ds_read_b128 v[172:175], v144 offset:22528\n\tv_cvt_pk_bf16_f32 v200, v56, v57\n\tv_cvt_pk_bf16_f32 v201, v58, v59\n\tv_cvt_pk_bf16_f32 v202, v60, v61\n\t" \
  "v_mfma_f32_16x16x32_bf16 v[4:7], v[112:115], v[100:103], v[4:7]\n\tds_read_b128 v[180:183], v145 offset:9216\n\t" \
  "buffer_store_dwordx4 v[200:203], v146, %0, %3 offen offset:0 sc1\n\tv_mfma_f32_16x16x32_bf16 v[8:11], v[112:115], v[104:107], v[8:11]\n\t" \
  "v_cvt_pk_bf16_f32 v204, v62, v63\n\tv_mfma_f32_16x16x32_bf16 v[12:15], v[112:115], v[108:111], v[12:15]\n\t" \
  "buffer_store_dwordx4 v[204:207], v146, %0, %3 offen offset:1024 sc1\n\ts_waitcnt lgkmcnt(6)\n\tv_mfma_f32_16x16x32_bf16 v[16:19], v[116:119], v[96:99], v[16:19]\n\t" \
  "ds_read_b128 v[64:67], v144 offset:17408\n\tds_read_b128 v[80:83], v145 offset:12288\n\ts_xor_b32 %1, %1, 0x8000\n\ts_xor_b32 %3, %3, 0x8000\n\t" \
  "v_mfma_f32_16x16x32_bf16 v[20:23], v[116:119], v[100:103], v[20:23]\n\tds_read_b128 v[68:71], v144 offset:19456\n\tds_read_b128 v[72:75], v144 offset:21504\n\t" \
  "v_mfma_f32_16x16x32_bf16 v[24:27], v[116:119], v[104:107], v[24:27]\n\tds_read_b128 v[76:79], v144 offset:23552\n\tds_read_b128 v[84:87], v145 offset:13312\n\t" \
  "v_mfma_f32_16x16x32_bf16 v[28:31], v[116:119], v[108:111], v[28:31]\n\ts_waitcnt lgkmcnt(7)\n\tv_mfma_f32_16x16x32_bf16 v[0:3], v[176:179], v[160:163], v[0:3]\n\t" \
  "s_barrier\n\tds_read_b128 v[96:99], v144 offset:32768\n\tv_mfma_f32_16x16x32_bf16 v[4:7], v[176:179], v[164:167], v[4:7]\n\tds_read_b128 v[112:115], v145 offset:16384\n\t" \
  "v_mfma_f32_16x16x32_bf16 v[8:11], v[176:179], v[168:171], v[8:11]\n\tds_read_b128 v[100:103], v144 offset:34816\n\t" \
  "v_mfma_f32_16x16x32_bf16 v[12:15], v[176:179], v[172:175], v[12:15]\n\tds_read_b128 v[104:107], v144 offset:36864\n\ts_waitcnt lgkmcnt(10)\n\t" \
  "v_mfma_f32_16x16x32_bf16 v[16:19], v[180:183], v[160:163], v[16:19]\n\tds_read_b128 v[108:111], v144 offset:38912\n\t" \
  "v_mfma_f32_16x16x32_bf16 v[20:23], v[180:183], v[164:167], v[20:23]\n\tds_read_b128 v[116:119], v145 offset:17408\n\t" \
  "v_mfma_f32_16x16x32_bf16 v[24:27], v[180:183], v[168:171], v[24:27]\n\tv_mfma_f32_16x16x32_bf16 v[28:31], v[180:183], v[172:175], v[28:31]\n\ts_waitcnt lgkmcnt(7)\n\t" \
  "v_mfma_f32_16x16x32_bf16 v[0:3], v[80:83], v[64:67], v[0:3]\n\tds_read_b128 v[160:163], v144 offset:33792\n\t" \
  "v_mfma_f32_16x16x32_bf16 v[4:7], v[80:83], v[68:71], v[4:7]\n\tds_read_b128 v[176:179], v145 offset:20480\n\t" \
  "v_mfma_f32_16x16x32_bf16 v[8:11], v[80:83], v[72:75], v[8:11]\n\tds_read_b128 v[164:167], v144 offset:35840\n\t" \
  "v_mfma_f32_16x16x32_bf16 v[12:15], v[80:83], v[76:79], v[12:15]\n\tds_read_b128 v[168:171], v144 offset:37888\n\ts_waitcnt lgkmcnt(10)\n\t" \
  "v_mfma_f32_16x16x32_bf16 v[16:19], v[84:87], v[64:67], v[16:19]\n\tds_read_b128 v[172:175], v144 offset:39936\n\t" \
  "v_mfma_f32_16x16x32_bf16 v[20:23], v[84:87], v[68:71], v[20:23]\n\tds_read_b128 v[180:183], v145 offset:21504\n\t" \
  "v_mfma_f32_16x16x32_bf16 v[24:27], v[84:87], v[72:75], v[24:27]\n\tv_mfma_f32_16x16x32_bf16 v[28:31], v[84:87], v[76:79], v[28:31]\n\ts_waitcnt lgkmcnt(6)\n\t" \
  "v_mfma_f32_16x16x32_bf16 v[0:3], v[112:115], v[96:99], v[0:3]\n\ts_barrier\n\tds_read_b128 v[64:67], v149 offset:0\n\t" \
  "v_mfma_f32_16x16x32_bf16 v[4:7], v[112:115], v[100:103], v[4:7]\n\tds_read_b128 v[80:83], v145 offset:24576\n\t" \
  "v_mfma_f32_16x16x32_bf16 v[8:11], v[112:115], v[104:107], v[8:11]\n\tds_read_b128 v[68:71], v149 offset:2048\n\t" \
  "v_mfma_f32_16x16x32_bf16 v[12:15], v[112:115], v[108:111], v[12:15]\n\tds_read_b128 v[72:75], v149 offset:4096\n\t" \
  "v_mfma_f32_16x16x32_bf16 v[16:19], v[116:119], v[96:99], v[16:19]\n\tds_read_b128 v[76:79], v149 offset:6144\n\t" \
  "v_mfma_f32_16x16x32_bf16 v[20:23], v[116:119], v[100:103], v[20:23]\n\tds_read_b128 v[84:87], v145 offset:25600\n\t" \
  "v_mfma_f32_16x16x32_bf16 v[24:27], v[116:119], v[104:107], v[24:27]\n\tv_mfma_f32_16x16x32_bf16 v[28:31], v[116:119], v[108:111], v[28:31]\n\ts_waitcnt lgkmcnt(6)\n\t" \
  "v_mfma_f32_16x16x32_bf16 v[0:3], v[176:179], v[160:163], v[0:3]\n\tds_read_b128 v[96:99], v149 offset:1024\n\t" \
  "v_mfma_f32_16x16x32_bf16 v[4:7], v[176:179], v[164:167], v[4:7]\n\tds_read_b128 v[112:115], v145 offset:28672\n\t" \
  "v_mfma_f32_16x16x32_bf16 v[8:11], v[176:179], v[168:171], v[8:11]\n\tds_read_b128 v[100:103], v149 offset:3072\n\t" \
  "v_mfma_f32_16x16x32_bf16 v[12:15], v[176:179], v[172:175], v[12:15]\n\tds_read_b128 v[104:107], v149 offset:5120\n\t" \
  "v_mfma_f32_16x16x32_bf16 v[16:19], v[180:183], v[160:163], v[16:19]\n\tds_read_b128 v[108:111], v149 offset:7168\n\t" \
  "v_mfma_f32_16x16x32_bf16 v[20:23], v[180:183], v[164:167], v[20:23]\n\tds_read_b128 v[116:119], v145 offset:29696\n\t" \
  "v_mfma_f32_16x16x32_bf16 v[24:27], v[180:183], v[168:171], v[24:27]\n\tv_mfma_f32_16x16x32_bf16 v[28:31], v[180:183], v[172:175], v[28:31]\n\ts_waitcnt lgkmcnt(6)\n\t" \
  "v_mfma_f32_16x16x32_bf16 v[0:3], v[80:83], v[64:67], v[0:3]\n\ts_barrier\n\tds_read_b128 v[160:163], v144 offset:49152\n\t" \
  "v_mfma_f32_16x16x32_bf16 v[4:7], v[80:83], v[68:71], v[4:7]\n\tds_read_b128 v[176:179], v145 offset:32768\n\t" \
  "v_mfma_f32_16x16x32_bf16 v[8:11], v[80:83], v[72:75], v[8:11]\n\tds_read_b128 v[164:167], v144 offset:51200\n\t" \
  "v_mfma_f32_16x16x32_bf16 v[12:15], v[80:83], v[76:79], v[12:15]\n\tds_read_b128 v[168:171], v144 offset:53248\n\t" \
  "v_mfma_f32_16x16x32_bf16 v[16:19], v[84:87], v[64:67], v[16:19]\n\tds_read_b128 v[172:175], v144 offset:55296\n\t" \
  "v_mfma_f32_16x16x32_bf16 v[20:23], v[84:87], v[68:71], v[20:23]\n\tds_read_b128 v[180:183], v145 offset:33792\n\t" \
  "v_mfma_f32_16x16x32_bf16 v[24:27], v[84:87], v[72:75], v[24:27]\n\tv_mfma_f32_16x16x32_bf16 v[28:31], v[84:87], v[76:79], v[28:31]\n\ts_waitcnt lgkmcnt(6)\n\t" \
  "v_mfma_f32_16x16x32_bf16 v[0:3], v[112:115], v[96:99], v[0:3]\n\tds_read_b128 v[64:67], v144 offset:50176\n\t" \
  "v_mfma_f32_16x16x32_bf16 v[4:7], v[112:115], v[100:103], v[4:7]\n\tds_read_b128 v[80:83], v145 offset:36864\n\t" \
  "v_mfma_f32_16x16x32_bf16 v[8:11], v[112:115], v[104:107], v[8:11]\n\tds_read_b128 v[68:71], v144 offset:52224\n\t" \
  "v_mfma_f32_16x16x32_bf16 v[12:15], v[112:115], v[108:111], v[12:15]\n\tds_read_b128 v[72:75], v144 offset:54272\n\t" \
  "v_mfma_f32_16x16x32_bf16 v[16:19], v[116:119], v[96:99], v[16:19]\n\tds_read_b128 v[76:79], v144 offset:56320\n\t" \
  "v_mfma_f32_16x16x32_bf16 v[20:23], v[116:119], v[100:103], v[20:23]\n\tds_read_b128 v[84:87], v145 offset:37888\n\t" \
  "v_mfma_f32_16x16x32_bf16 v[24:27], v[116:119], v[104:107], v[24:27]\n\tv_mfma_f32_16x16x32_bf16 v[28:31], v[116:119], v[108:111], v[28:31]\n\ts_waitcnt lgkmcnt(6)\n\t" \
  "v_mfma_f32_16x16x32_bf16 v[0:3], v[176:179], v[160:163], v[0:3]\n\ts_barrier\n\tds_read_b128 v[96:99], v144 offset:0\n\t" \
  "v_mfma_f32_16x16x32_bf16 v[4:7], v[176:179], v[164:167], v[4:7]\n\tds_read_b128 v[112:115], v145 offset:40960\n\t" \
  "v_mfma_f32_16x16x32_bf16 v[8:11], v[176:179], v[168:171], v[8:11]\n\tds_read_b128 v[100:103], v144 offset:2048\n\t" \
  "v_mfma_f32_16x16x32_bf16 v[12:15], v[176:179], v[172:175], v[12:15]\n\tds_read_b128 v[104:107], v144 offset:4096\n\t" \
  "v_mfma_f32_16x16x32_bf16 v[16:19], v[180:183], v[160:163], v[16:19]\n\tds_read_b128 v[108:111], v144 offset:6144\n\t" \
  "v_mfma_f32_16x16x32_bf16 v[20:23], v[180:183], v[164:167], v[20:23]\n\tds_read_b128 v[116:119], v145 offset:41984\n\t" \
  "v_mfma_f32_16x16x32_bf16 v[24:27], v[180:183], v[168:171], v[24:27]\n\tv_mfma_f32_16x16x32_bf16 v[28:31], v[180:183], v[172:175], v[28:31]\n\ts_waitcnt lgkmcnt(6)\n\t" \
  "v_mfma_f32_16x16x32_bf16 v[0:3], v[80:83], v[64:67], v[0:3]\n\tds_read_b128 v[160:163], v144 offset:1024\n\t" \
  "v_mfma_f32_16x16x32_bf16 v[4:7], v[80:83], v[68:71], v[4:7]\n\tds_read_b128 v[176:179], v145 offset:45056\n\t" \
  "v_mfma_f32_16x16x32_bf16 v[8:11], v[80:83], v[72:75], v[8:11]\n\tds_read_b128 v[164:167], v144 offset:3072\n\t" \
  "v_mfma_f32_16x16x32_bf16 v[12:15], v[80:83], v[76:79], v[12:15]\n\tds_read_b128 v[168:171], v144 offset:5120\n\t" \
  "v_mfma_f32_16x16x32_bf16 v[16:19], v[84:87], v[64:67], v[16:19]\n\tds_read_b128 v[172:175], v144 offset:7168\n\t" \
  "v_mfma_f32_16x16x32_bf16 v[20:23], v[84:87], v[68:71], v[20:23]\n\tds_read_b128 v[180:183], v145 offset:46080\n\t" \
  "v_mfma_f32_16x16x32_bf16 v[24:27], v[84:87], v[72:75], v[24:27]\n\tv_mfma_f32_16x16x32_bf16 v[28:31], v[84:87], v[76:79], v[28:31]\n\ts_waitcnt lgkmcnt(6)\n\t" \
  "v_mfma_f32_16x16x32_bf16 v[0:3], v[112:115], v[96:99], v[0:3]\n\ts_barrier\n\tds_read_b128 v[64:67], v144 offset:16384\n\t" \
  "v_mfma_f32_16x16x32_bf16 v[4:7], v[112:115], v[100:103], v[4:7]\n\tds_read_b128 v[80:83], v145 offset:49152\n\t" \
  "v_mfma_f32_16x16x32_bf16 v[8:11], v[112:115], v[104:107], v[8:11]\n\tds_read_b128 v[68:71], v144 offset:18432\n\t" \
  "v_mfma_f32_16x16x32_bf16 v[12:15], v[112:115], v[108:111], v[12:15]\n\tds_read_b128 v[72:75], v144 offset:20480\n\t" \
  "v_mfma_f32_16x16x32_bf16 v[16:19], v[116:119], v[96:99], v[16:19]\n\tds_read_b128 v[76:79], v144 offset:22528\n\t" \
  "v_mfma_f32_16x16x32_bf16 v[20:23], v[116:119], v[100:103], v[20:23]\n\tds_read_b128 v[84:87], v145 offset:50176\n\t" \
  "v_mfma_f32_16x16x32_bf16 v[24:27], v[116:119], v[104:107], v[24:27]\n\tv_mfma_f32_16x16x32_bf16 v[28:31], v[116:119], v[108:111], v[28:31]\n\ts_waitcnt lgkmcnt(6)\n\t" \
  "v_mfma_f32_16x16x32_bf16 v[0:3], v[176:179], v[160:163], v[0:3]\n\tds_read_b128 v[96:99], v144 offset:17408\n\t" \
  "buffer_load_dwordx4 v[32:35], v146, %0, %1 offen offset:0 sc1\n\tv_mfma_f32_16x16x32_bf16 v[4:7], v[176:179], v[164:167], v[4:7]\n\t" \
  "ds_read_b128 v[112:115], v145 offset:53248\n\tbuffer_load_dwordx4 v[36:39], v146, %0, %1 offen offset:1024 sc1\n\t" \
  "v_mfma_f32_16x16x32_bf16 v[8:11], v[176:179], v[168:171], v[8:11]\n\tds_read_b128 v[100:103], v144 offset:19456\n\t" \
  "buffer_load_dwordx4 v[40:43], v146, %0, %1 offen offset:2048 sc1\n\tv_mfma_f32_16x16x32_bf16 v[12:15], v[176:179], v[172:175], v[12:15]\n\t" \
  "ds_read_b128 v[104:107], v144 offset:21504\n\tbuffer_load_dwordx4 v[44:47], v146, %0, %1 offen offset:3072 sc1\n\t" \
  "v_mfma_f32_16x16x32_bf16 v[16:19], v[180:183], v[160:163], v[16:19]\n\tds_read_b128 v[108:111], v144 offset:23552\n\t" \
  "buffer_load_dwordx4 v[48:51], v146, %0, %3 offen offset:0 sc1\n\tv_mfma_f32_16x16x32_bf16 v[20:23], v[180:183], v[164:167], v[20:23]\n\t" \
  "ds_read_b128 v[116:119], v145 offset:54272\n\tbuffer_load_dwordx4 v[52:55], v146, %0, %3 offen offset:1024 sc1\n\t" \
  "v_mfma_f32_16x16x32_bf16 v[24:27], v[180:183], v[168:171], v[24:27]\n\tv_mfma_f32_16x16x32_bf16 v[28:31], v[180:183], v[172:175], v[28:31]\n\ts_waitcnt lgkmcnt(6)\n\t" \
  "v_mfma_f32_16x16x32_bf16 v[0:3], v[80:83], v[64:67], v[0:3]\n\ts_barrier\n\tds_read_b128 v[160:163], v144 offset:32768\n\t" \
  "v_mfma_f32_16x16x32_bf16 v[4:7], v[80:83], v[68:71], v[4:7]\n\tds_read_b128 v[176:179], v145 offset:57344\n\t" \
  "v_mfma_f32_16x16x32_bf16 v[8:11], v[80:83], v[72:75], v[8:11]\n\tds_read_b128 v[164:167], v144 offset:34816\n\t" \
  "v_mfma_f32_16x16x32_bf16 v[12:15], v[80:83], v[76:79], v[12:15]\n\tds_read_b128 v[168:171], v144 offset:36864\n\t" \
  "v_mfma_f32_16x16x32_bf16 v[16:19], v[84:87], v[64:67], v[16:19]\n\tds_read_b128 v[172:175], v144 offset:38912\n\t" \
  "v_mfma_f32_16x16x32_bf16 v[20:23], v[84:87], v[68:71], v[20:23]\n\tds_read_b128 v[180:183], v145 offset:58368\n\t" \
  "v_mfma_f32_16x16x32_bf16 v[24:27], v[84:87], v[72:75], v[24:27]\n\tv_mfma_f32_16x16x32_bf16 v[28:31], v[84:87], v[76:79], v[28:31]\n\ts_waitcnt lgkmcnt(6)\n\t" \
  "v_mfma_f32_16x16x32_bf16 v[0:3], v[112:115], v[96:99], v[0:3]\n\tds_read_b128 v[64:67], v144 offset:33792\n\t" \
  "v_mfma_f32_16x16x32_bf16 v[4:7], v[112:115], v[100:103], v[4:7]\n\tds_read_b128 v[80:83], v145 offset:61440\n\t" \
  "v_mfma_f32_16x16x32_bf16 v[8:11], v[112:115], v[104:107], v[8:11]\n\tds_read_b128 v[68:71], v144 offset:35840\n\t" \
  "v_mfma_f32_16x16x32_bf16 v[12:15], v[112:115], v[108:111], v[12:15]\n\tds_read_b128 v[72:75], v144 offset:37888\n\t" \
  "v_mfma_f32_16x16x32_bf16 v[16:19], v[116:119], v[96:99], v[16:19]\n\tds_read_b128 v[76:79], v144 offset:39936\n\t" \
  "v_mfma_f32_16x16x32_bf16 v[20:23], v[116:119], v[100:103], v[20:23]\n\tds_read_b128 v[84:87], v145 offset:62464\n\t" \
  "v_mfma_f32_16x16x32_bf16 v[24:27], v[116:119], v[104:107], v[24:27]\n\tv_mfma_f32_16x16x32_bf16 v[28:31], v[116:119], v[108:111], v[28:31]\n\ts_waitcnt lgkmcnt(6)\n\t" \
  "v_mfma_f32_16x16x32_bf16 v[0:3], v[176:179], v[160:163], v[0:3]\n\tv_mfma_f32_16x16x32_bf16 v[4:7], v[176:179], v[164:167], v[4:7]\n\t" \
  "v_mfma_f32_16x16x32_bf16 v[8:11], v[176:179], v[168:171], v[8:11]\n\tv_mfma_f32_16x16x32_bf16 v[12:15], v[176:179], v[172:175], v[12:15]\n\t" \
  "v_mfma_f32_16x16x32_bf16 v[16:19], v[180:183], v[160:163], v[16:19]\n\ts_waitcnt lgkmcnt(0)\n\ts_barrier\n\t" \
  "v_mfma_f32_16x16x32_bf16 v[20:23], v[180:183], v[164:167], v[20:23]\n\tv_mfma_f32_16x16x32_bf16 v[24:27], v[180:183], v[168:171], v[24:27]\n\t" \
  "v_mfma_f32_16x16x32_bf16 v[28:31], v[180:183], v[172:175], v[28:31]\n\tv_mfma_f32_16x16x32_bf16 v[0:3], v[80:83], v[64:67], v[0:3]\n\t" \
  "v_mfma_f32_16x16x32_bf16 v[4:7], v[80:83], v[68:71], v[4:7]\n\tv_mfma_f32_16x16x32_bf16 v[8:11], v[80:83], v[72:75], v[8:11]\n\t" \
  "v_mfma_f32_16x16x32_bf16 v[12:15], v[80:83], v[76:79], v[12:15]\n\tv_mfma_f32_16x16x32_bf16 v[16:19], v[84:87], v[64:67], v[16:19]\n\t" \
  "v_mfma_f32_16x16x32_bf16 v[20:23], v[84:87], v[68:71], v[20:23]\n\tv_mfma_f32_16x16x32_bf16 v[24:27], v[84:87], v[72:75], v[24:27]\n\t" \
  "v_mfma_f32_16x16x32_bf16 v[28:31], v[84:87], v[76:79], v[28:31]\n\ts_nop 7\n\t"
#define FFN2_CLOB \
  "v64", "v65", "v66", "v67", "v68", "v69", "v70", "v71", "v72", "v73", "v74", "v75", "v76", "v77", "v78", "v79", "v80", "v81", "v82", "v83", "v84", "v85", "v86", "v87", \
  "v88", "v89", "v90", "v91", "v92", "v93", "v94", "v95", "v96", "v97", "v98", "v99", "v100", "v101", "v102", "v103", "v104", "v105", "v106", "v107", "v108", "v109", \
  "v110", "v111", "v112", "v113", "v114", "v115", "v116", "v117", "v118", "v119", "v120", "v121", "v122", "v123", "v124", "v125", "v126", "v127", "v160", "v161", "v162", \
  "v163", "v164", "v165", "v166", "v167", "v168", "v169", "v170", "v171", "v172", "v173", "v174", "v175", "v176", "v177", "v178", "v179", "v180", "v181", "v182", "v183", \
  "v184", "v185", "v186", "v187", "v188", "v189", "v190", "v191", "v192", "v193", "v194", "v195", "v196", "v197", "v198", "v199", "v200", "v201", "v202", "v203", "v204", \
  "v205", "v206", "v207"
#define FFN2_LOAD \
  "s_nop 1\n\tv_readlane_b32 %9, v6, 2\n\ts_nop 3\n\ts_add_u32 m0, %3, 98304\n\ts_nop 0\n\tbuffer_load_dwordx4 v0, %1, %9 offen offset:0 sc1 lds\n\t" \
  "buffer_load_dwordx4 v0, %1, %9 offen offset:1024 sc1 lds\n\tbuffer_load_dwordx4 v0, %1, %9 offen offset:2048 sc1 lds\n\t" \
  "buffer_load_dwordx4 v0, %1, %9 offen offset:3072 sc1 lds\n\tv_readlane_b32 %9, v6, 3\n\ts_nop 3\n\ts_add_u32 m0, %3, 114688\n\ts_nop 0\n\t" \
  "buffer_load_dwordx4 v0, %1, %9 offen offset:0 sc1 lds\n\tbuffer_load_dwordx4 v0, %1, %9 offen offset:1024 sc1 lds\n\t" \
  "buffer_load_dwordx4 v0, %1, %9 offen offset:2048 sc1 lds\n\tbuffer_load_dwordx4 v0, %1, %9 offen offset:3072 sc1 lds\n\tv_readlane_b32 %8, v5, 3\n\t" \
  "v_readlane_b32 %9, v6, 4\n\ts_waitcnt vmcnt(0)\n\ts_sleep 56\n\tbuffer_load_dwordx2 v[16:17], v7, %2, 0 offen sc1\n\ts_waitcnt lgkmcnt(0)\n\ts_barrier\n\t" \
  "s_mov_b32 %10, 0\n\ts_waitcnt lgkmcnt(0)\n\ts_barrier\n\ts_cmp_eq_u32 %10, 0\n\ts_cbranch_scc0 .Lkb%=\n\ts_waitcnt vmcnt(0)\n\tv_cmp_eq_u64 vcc, %5, v[16:17]\n\t" \
  "s_lshr_b32 %6, vcc_lo, 8\n\ts_and_b32 %6, %6, vcc_lo\n\ts_bitcmp1_b32 %6, 1\n\ts_cbranch_scc1 .Lk1x%=\n\ts_movk_i32 %11, 1\n\ts_call_b64 %7, .Lks%=\n\t.Lk1x%=:\n\t" \
  "s_add_u32 m0, %3, 49152\n\ts_nop 0\n\tbuffer_load_dwordx4 v0, %0, %8 offen offset:0 sc1 lds\n\tbuffer_load_dwordx4 v0, %0, %8 offen offset:1024 sc1 lds\n\t" \
  "buffer_load_dwordx4 v0, %0, %8 offen offset:2048 sc1 lds\n\tbuffer_load_dwordx4 v0, %0, %8 offen offset:3072 sc1 lds\n\t.Lkb%=:\n\tv_readlane_b32 %8, v5, 4\n\ts_nop 1\n\t" \
  "s_bitcmp1_b32 %6, 2\n\ts_cbranch_scc1 .Lk2%=\n\ts_movk_i32 %11, 2\n\ts_call_b64 %7, .Lks%=\n\t.Lk2%=:\n\ts_add_u32 m0, %3, 0\n\ts_nop 0\n\t" \
  "buffer_load_dwordx4 v0, %0, %8 offen offset:0 sc1 lds\n\tbuffer_load_dwordx4 v0, %0, %8 offen offset:1024 sc1 lds\n\t" \
  "buffer_load_dwordx4 v0, %0, %8 offen offset:2048 sc1 lds\n\tbuffer_load_dwordx4 v0, %0, %8 offen offset:3072 sc1 lds\n\ts_add_u32 m0, %3, 65536\n\ts_nop 0\n\t" \
  "buffer_load_dwordx4 v0, %1, %9 offen offset:0 sc1 lds\n\tbuffer_load_dwordx4 v0, %1, %9 offen offset:1024 sc1 lds\n\t" \
  "buffer_load_dwordx4 v0, %1, %9 offen offset:2048 sc1 lds\n\tbuffer_load_dwordx4 v0, %1, %9 offen offset:3072 sc1 lds\n\tv_readlane_b32 %8, v5, 5\n\t" \
  "v_readlane_b32 %9, v6, 5\n\ts_waitcnt lgkmcnt(0)\n\ts_barrier\n\ts_bitcmp1_b32 %6, 3\n\ts_cbranch_scc1 .Lk3%=\n\ts_movk_i32 %11, 3\n\ts_call_b64 %7, .Lks%=\n\t.Lk3%=:\n\t" \
  "s_add_u32 m0, %3, 16384\n\ts_nop 0\n\tbuffer_load_dwordx4 v0, %0, %8 offen offset:0 sc1 lds\n\tbuffer_load_dwordx4 v0, %0, %8 offen offset:1024 sc1 lds\n\t" \
  "buffer_load_dwordx4 v0, %0, %8 offen offset:2048 sc1 lds\n\tbuffer_load_dwordx4 v0, %0, %8 offen offset:3072 sc1 lds\n\ts_add_u32 m0, %3, 81920\n\ts_nop 0\n\t" \
  "buffer_load_dwordx4 v0, %1, %9 offen offset:0 sc1 lds\n\tbuffer_load_dwordx4 v0, %1, %9 offen offset:1024 sc1 lds\n\t" \
  "buffer_load_dwordx4 v0, %1, %9 offen offset:2048 sc1 lds\n\tbuffer_load_dwordx4 v0, %1, %9 offen offset:3072 sc1 lds\n\tv_readlane_b32 %8, v5, 6\n\t" \
  "v_readlane_b32 %9, v6, 6\n\ts_waitcnt vmcnt(16) lgkmcnt(0)\n\ts_barrier\n\tbuffer_store_dwordx2 v[12:13], v9, %2, 0 offen sc1\n\ts_bitcmp1_b32 %6, 4\n\t" \
  "s_cbranch_scc1 .Lk4%=\n\ts_movk_i32 %11, 4\n\ts_call_b64 %7, .Lks%=\n\t.Lk4%=:\n\ts_add_u32 m0, %3, 32768\n\ts_nop 0\n\t" \
  "buffer_load_dwordx4 v0, %0, %8 offen offset:0 sc1 lds\n\tbuffer_load_dwordx4 v0, %0, %8 offen offset:1024 sc1 lds\n\t" \
  "buffer_load_dwordx4 v0, %0, %8 offen offset:2048 sc1 lds\n\tbuffer_load_dwordx4 v0, %0, %8 offen offset:3072 sc1 lds\n\ts_add_u32 m0, %3, 98304\n\ts_nop 0\n\t" \
  "buffer_load_dwordx4 v0, %1, %9 offen offset:0 sc1 lds\n\tbuffer_load_dwordx4 v0, %1, %9 offen offset:1024 sc1 lds\n\t" \
  "buffer_load_dwordx4 v0, %1, %9 offen offset:2048 sc1 lds\n\tbuffer_load_dwordx4 v0, %1, %9 offen offset:3072 sc1 lds\n\tv_readlane_b32 %8, v5, 7\n\t" \
  "v_readlane_b32 %9, v6, 7\n\ts_waitcnt vmcnt(17) lgkmcnt(0)\n\ts_barrier\n\tv_cmp_eq_u64 vcc, %5, v[16:17]\n\ts_lshr_b32 %6, vcc_lo, 8\n\ts_and_b32 %6, %6, vcc_lo\n\t" \
  "s_bitcmp1_b32 %6, 5\n\ts_cbranch_scc1 .Lk5%=\n\ts_movk_i32 %11, 5\n\ts_call_b64 %7, .Lks%=\n\t.Lk5%=:\n\ts_add_u32 m0, %3, 49152\n\ts_nop 0\n\t" \
  "buffer_load_dwordx4 v0, %0, %8 offen offset:0 sc1 lds\n\tbuffer_load_dwordx4 v0, %0, %8 offen offset:1024 sc1 lds\n\t" \
  "buffer_load_dwordx4 v0, %0, %8 offen offset:2048 sc1 lds\n\tbuffer_load_dwordx4 v0, %0, %8 offen offset:3072 sc1 lds\n\ts_add_u32 m0, %3, 114688\n\ts_nop 0\n\t" \
  "buffer_load_dwordx4 v0, %1, %9 offen offset:0 sc1 lds\n\tbuffer_load_dwordx4 v0, %1, %9 offen offset:1024 sc1 lds\n\t" \
  "buffer_load_dwordx4 v0, %1, %9 offen offset:2048 sc1 lds\n\tbuffer_load_dwordx4 v0, %1, %9 offen offset:3072 sc1 lds\n\tv_readlane_b32 %8, v5, 8\n\t" \
  "v_readlane_b32 %9, v6, 8\n\ts_waitcnt vmcnt(17) lgkmcnt(0)\n\ts_barrier\n\ts_bitcmp1_b32 %6, 6\n\ts_cbranch_scc1 .Lk6%=\n\ts_movk_i32 %11, 6\n\ts_call_b64 %7, .Lks%=\n\t" \
  ".Lk6%=:\n\tbuffer_load_dwordx2 v[18:19], v7, %2, 0 offen offset:512 sc1\n\ts_add_u32 m0, %3, 0\n\ts_nop 0\n\tbuffer_load_dwordx4 v0, %0, %8 offen offset:0 sc1 lds\n\t" \
  "buffer_load_dwordx4 v0, %0, %8 offen offset:1024 sc1 lds\n\tbuffer_load_dwordx4 v0, %0, %8 offen offset:2048 sc1 lds\n\t" \
  "buffer_load_dwordx4 v0, %0, %8 offen offset:3072 sc1 lds\n\ts_add_u32 m0, %3, 65536\n\ts_nop 0\n\tbuffer_load_dwordx4 v0, %1, %9 offen offset:0 sc1 lds\n\t" \
  "buffer_load_dwordx4 v0, %1, %9 offen offset:1024 sc1 lds\n\tbuffer_load_dwordx4 v0, %1, %9 offen offset:2048 sc1 lds\n\t" \
  "buffer_load_dwordx4 v0, %1, %9 offen offset:3072 sc1 lds\n\tv_readlane_b32 %8, v5, 9\n\tv_readlane_b32 %9, v6, 9\n\ts_waitcnt vmcnt(17) lgkmcnt(0)\n\ts_barrier\n\t" \
  "s_bitcmp1_b32 %6, 7\n\ts_cbranch_scc1 .Lk7%=\n\ts_movk_i32 %11, 7\n\ts_call_b64 %7, .Lks%=\n\t.Lk7%=:\n\ts_add_u32 m0, %3, 16384\n\ts_nop 0\n\t" \
  "buffer_load_dwordx4 v0, %0, %8 offen offset:0 sc1 lds\n\tbuffer_load_dwordx4 v0, %0, %8 offen offset:1024 sc1 lds\n\t" \
  "buffer_load_dwordx4 v0, %0, %8 offen offset:2048 sc1 lds\n\tbuffer_load_dwordx4 v0, %0, %8 offen offset:3072 sc1 lds\n\ts_add_u32 m0, %3, 81920\n\ts_nop 0\n\t" \
  "buffer_load_dwordx4 v0, %1, %9 offen offset:0 sc1 lds\n\tbuffer_load_dwordx4 v0, %1, %9 offen offset:1024 sc1 lds\n\t" \
  "buffer_load_dwordx4 v0, %1, %9 offen offset:2048 sc1 lds\n\tbuffer_load_dwordx4 v0, %1, %9 offen offset:3072 sc1 lds\n\tv_readlane_b32 %8, v5, 10\n\t" \
  "v_readlane_b32 %9, v6, 10\n\ts_waitcnt vmcnt(16) lgkmcnt(0)\n\ts_barrier\n\tv_add_u32 v7, 512, v7\n\tv_cmp_eq_u64 vcc, %5, v[18:19]\n\ts_lshr_b32 %6, vcc_lo, 8\n\t" \
  "s_and_b32 %6, %6, vcc_lo\n\ts_bitcmp1_b32 %6, 1\n\ts_cbranch_scc1 .Lk1m%=\n\ts_movk_i32 %11, 1\n\ts_call_b64 %7, .Lks%=\n\t.Lk1m%=:\n\ts_add_u32 m0, %3, 32768\n\t" \
  "s_nop 0\n\tbuffer_load_dwordx4 v0, %0, %8 offen offset:0 sc1 lds\n\tbuffer_load_dwordx4 v0, %0, %8 offen offset:1024 sc1 lds\n\t" \
  "buffer_load_dwordx4 v0, %0, %8 offen offset:2048 sc1 lds\n\tbuffer_load_dwordx4 v0, %0, %8 offen offset:3072 sc1 lds\n\ts_add_u32 m0, %3, 98304\n\ts_nop 0\n\t" \
  "buffer_load_dwordx4 v0, %1, %9 offen offset:0 sc1 lds\n\tbuffer_load_dwordx4 v0, %1, %9 offen offset:1024 sc1 lds\n\t" \
  "buffer_load_dwordx4 v0, %1, %9 offen offset:2048 sc1 lds\n\tbuffer_load_dwordx4 v0, %1, %9 offen offset:3072 sc1 lds\n\tv_readlane_b32 %8, v5, 11\n\t" \
  "v_readlane_b32 %9, v6, 11\n\ts_waitcnt vmcnt(16) lgkmcnt(0)\n\ts_barrier\n\ts_bitcmp1_b32 %6, 2\n\ts_cbranch_scc1 .Lk2m%=\n\ts_movk_i32 %11, 2\n\t" \
  "s_call_b64 %7, .Lks%=\n\t.Lk2m%=:\n\ts_add_u32 m0, %3, 49152\n\ts_nop 0\n\tbuffer_load_dwordx4 v0, %0, %8 offen offset:0 sc1 lds\n\t" \
  "buffer_load_dwordx4 v0, %0, %8 offen offset:1024 sc1 lds\n\tbuffer_load_dwordx4 v0, %0, %8 offen offset:2048 sc1 lds\n\t" \
  "buffer_load_dwordx4 v0, %0, %8 offen offset:3072 sc1 lds\n\ts_add_u32 m0, %3, 114688\n\ts_nop 0\n\tbuffer_load_dwordx4 v0, %1, %9 offen offset:0 sc1 lds\n\t" \
  "buffer_load_dwordx4 v0, %1, %9 offen offset:1024 sc1 lds\n\tbuffer_load_dwordx4 v0, %1, %9 offen offset:2048 sc1 lds\n\t" \
  "buffer_load_dwordx4 v0, %1, %9 offen offset:3072 sc1 lds\n\tv_readlane_b32 %8, v5, 12\n\tv_readlane_b32 %9, v6, 12\n\ts_waitcnt vmcnt(16) lgkmcnt(0)\n\ts_barrier\n\t" \
  "s_bitcmp1_b32 %6, 3\n\ts_cbranch_scc1 .Lk3m%=\n\ts_movk_i32 %11, 3\n\ts_call_b64 %7, .Lks%=\n\t.Lk3m%=:\n\tbuffer_load_dwordx2 v[18:19], v8, %2, 0 offen sc1\n\t" \
  "s_add_u32 m0, %3, 0\n\ts_nop 0\n\tbuffer_load_dwordx4 v0, %0, %8 offen offset:0 sc1 lds\n\tbuffer_load_dwordx4 v0, %0, %8 offen offset:1024 sc1 lds\n\t" \
  "buffer_load_dwordx4 v0, %0, %8 offen offset:2048 sc1 lds\n\tbuffer_load_dwordx4 v0, %0, %8 offen offset:3072 sc1 lds\n\ts_add_u32 m0, %3, 65536\n\ts_nop 0\n\t" \
  "buffer_load_dwordx4 v0, %1, %9 offen offset:0 sc1 lds\n\tbuffer_load_dwordx4 v0, %1, %9 offen offset:1024 sc1 lds\n\t" \
  "buffer_load_dwordx4 v0, %1, %9 offen offset:2048 sc1 lds\n\tbuffer_load_dwordx4 v0, %1, %9 offen offset:3072 sc1 lds\n\tv_readlane_b32 %8, v5, 13\n\t" \
  "v_readlane_b32 %9, v6, 13\n\ts_waitcnt vmcnt(17) lgkmcnt(0)\n\ts_barrier\n\ts_bitcmp1_b32 %6, 4\n\ts_cbranch_scc1 .Lk4m%=\n\ts_movk_i32 %11, 4\n\t" \
  "s_call_b64 %7, .Lks%=\n\t.Lk4m%=:\n\ts_add_u32 m0, %3, 16384\n\ts_nop 0\n\tbuffer_load_dwordx4 v0, %0, %8 offen offset:0 sc1 lds\n\t" \
  "buffer_load_dwordx4 v0, %0, %8 offen offset:1024 sc1 lds\n\tbuffer_load_dwordx4 v0, %0, %8 offen offset:2048 sc1 lds\n\t" \
  "buffer_load_dwordx4 v0, %0, %8 offen offset:3072 sc1 lds\n\ts_add_u32 m0, %3, 81920\n\ts_nop 0\n\tbuffer_load_dwordx4 v0, %1, %9 offen offset:0 sc1 lds\n\t" \
  "buffer_load_dwordx4 v0, %1, %9 offen offset:1024 sc1 lds\n\tbuffer_load_dwordx4 v0, %1, %9 offen offset:2048 sc1 lds\n\t" \
  "buffer_load_dwordx4 v0, %1, %9 offen offset:3072 sc1 lds\n\tv_readlane_b32 %8, v5, 14\n\tv_readlane_b32 %9, v6, 14\n\ts_waitcnt vmcnt(16) lgkmcnt(0)\n\ts_barrier\n\t" \
  "s_bitcmp1_b32 %6, 5\n\ts_cbranch_scc1 .Lk5m%=\n\ts_movk_i32 %11, 5\n\ts_call_b64 %7, .Lks%=\n\t.Lk5m%=:\n\ts_mov_b32 %11, 0x20000\n\t.Lkl%=:\n\t" \
  "v_cmp_eq_u64 vcc, %5, v[18:19]\n\ts_cmp_eq_u64 vcc, 0xffff\n\ts_cbranch_scc0 .Lkm%=\n\t.Lkn%=:\n\ts_add_u32 m0, %3, 32768\n\ts_nop 0\n\t" \
  "buffer_load_dwordx4 v0, %0, %8 offen offset:0 sc1 lds\n\tbuffer_load_dwordx4 v0, %0, %8 offen offset:1024 sc1 lds\n\t" \
  "buffer_load_dwordx4 v0, %0, %8 offen offset:2048 sc1 lds\n\tbuffer_load_dwordx4 v0, %0, %8 offen offset:3072 sc1 lds\n\ts_add_u32 m0, %3, 98304\n\ts_nop 0\n\t" \
  "buffer_load_dwordx4 v0, %1, %9 offen offset:0 sc1 lds\n\tbuffer_load_dwordx4 v0, %1, %9 offen offset:1024 sc1 lds\n\t" \
  "buffer_load_dwordx4 v0, %1, %9 offen offset:2048 sc1 lds\n\tbuffer_load_dwordx4 v0, %1, %9 offen offset:3072 sc1 lds\n\tv_readlane_b32 %8, v5, 15\n\t" \
  "v_readlane_b32 %9, v6, 15\n\ts_waitcnt vmcnt(16) lgkmcnt(0)\n\ts_barrier\n\ts_bitcmp1_b32 %6, 6\n\ts_cbranch_scc1 .Lk6m%=\n\ts_movk_i32 %11, 6\n\t" \
  "s_call_b64 %7, .Lks%=\n\t.Lk6m%=:\n\ts_add_u32 m0, %3, 49152\n\ts_nop 0\n\tbuffer_load_dwordx4 v0, %0, %8 offen offset:0 sc1 lds\n\t" \
  "buffer_load_dwordx4 v0, %0, %8 offen offset:1024 sc1 lds\n\tbuffer_load_dwordx4 v0, %0, %8 offen offset:2048 sc1 lds\n\t" \
  "buffer_load_dwordx4 v0, %0, %8 offen offset:3072 sc1 lds\n\ts_add_u32 m0, %3, 114688\n\ts_nop 0\n\tbuffer_load_dwordx4 v0, %1, %9 offen offset:0 sc1 lds\n\t" \
  "buffer_load_dwordx4 v0, %1, %9 offen offset:1024 sc1 lds\n\tbuffer_load_dwordx4 v0, %1, %9 offen offset:2048 sc1 lds\n\t" \
  "buffer_load_dwordx4 v0, %1, %9 offen offset:3072 sc1 lds\n\tv_readlane_b32 %8, v5, 16\n\tv_readlane_b32 %9, v6, 16\n\ts_waitcnt vmcnt(16) lgkmcnt(0)\n\ts_barrier\n\t" \
  "s_bitcmp1_b32 %6, 7\n\ts_cbranch_scc1 .Lk7m%=\n\ts_movk_i32 %11, 7\n\ts_call_b64 %7, .Lks%=\n\t.Lk7m%=:\n\ts_add_u32 m0, %3, 0\n\ts_nop 0\n\t" \
  "buffer_load_dwordx4 v0, %0, %8 offen offset:0 sc1 lds\n\tbuffer_load_dwordx4 v0, %0, %8 offen offset:1024 sc1 lds\n\t" \
  "buffer_load_dwordx4 v0, %0, %8 offen offset:2048 sc1 lds\n\tbuffer_load_dwordx4 v0, %0, %8 offen offset:3072 sc1 lds\n\ts_add_u32 m0, %3, 65536\n\ts_nop 0\n\t" \
  "buffer_load_dwordx4 v0, %1, %9 offen offset:0 sc1 lds\n\tbuffer_load_dwordx4 v0, %1, %9 offen offset:1024 sc1 lds\n\t" \
  "buffer_load_dwordx4 v0, %1, %9 offen offset:2048 sc1 lds\n\tbuffer_load_dwordx4 v0, %1, %9 offen offset:3072 sc1 lds\n\tv_readlane_b32 %8, v5, 17\n\t" \
  "v_readlane_b32 %9, v6, 17\n\ts_waitcnt vmcnt(16) lgkmcnt(0)\n\ts_barrier\n\ts_add_u32 m0, %3, 16384\n\ts_nop 0\n\t" \
  "buffer_load_dwordx4 v0, %0, %8 offen offset:0 sc1 lds\n\tbuffer_load_dwordx4 v0, %0, %8 offen offset:1024 sc1 lds\n\t" \
  "buffer_load_dwordx4 v0, %0, %8 offen offset:2048 sc1 lds\n\tbuffer_load_dwordx4 v0, %0, %8 offen offset:3072 sc1 lds\n\ts_add_u32 m0, %3, 81920\n\ts_nop 0\n\t" \
  "buffer_load_dwordx4 v0, %1, %9 offen offset:0 sc1 lds\n\tbuffer_load_dwordx4 v0, %1, %9 offen offset:1024 sc1 lds\n\t" \
  "buffer_load_dwordx4 v0, %1, %9 offen offset:2048 sc1 lds\n\tbuffer_load_dwordx4 v0, %1, %9 offen offset:3072 sc1 lds\n\tv_readlane_b32 %8, v5, 18\n\t" \
  "v_readlane_b32 %9, v6, 18\n\ts_waitcnt vmcnt(16) lgkmcnt(0)\n\ts_barrier\n\ts_add_u32 m0, %3, 32768\n\ts_nop 0\n\t" \
  "buffer_load_dwordx4 v0, %0, %8 offen offset:0 sc1 lds\n\tbuffer_load_dwordx4 v0, %0, %8 offen offset:1024 sc1 lds\n\t" \
  "buffer_load_dwordx4 v0, %0, %8 offen offset:2048 sc1 lds\n\tbuffer_load_dwordx4 v0, %0, %8 offen offset:3072 sc1 lds\n\ts_add_u32 m0, %3, 98304\n\ts_nop 0\n\t" \
  "buffer_load_dwordx4 v0, %1, %9 offen offset:0 sc1 lds\n\tbuffer_load_dwordx4 v0, %1, %9 offen offset:1024 sc1 lds\n\t" \
  "buffer_load_dwordx4 v0, %1, %9 offen offset:2048 sc1 lds\n\tbuffer_load_dwordx4 v0, %1, %9 offen offset:3072 sc1 lds\n\tv_readlane_b32 %8, v5, 19\n\t" \
  "v_readlane_b32 %9, v6, 19\n\ts_waitcnt vmcnt(16) lgkmcnt(0)\n\ts_barrier\n\ts_add_u32 m0, %3, 49152\n\ts_nop 0\n\t" \
  "buffer_load_dwordx4 v0, %0, %8 offen offset:0 sc1 lds\n\tbuffer_load_dwordx4 v0, %0, %8 offen offset:1024 sc1 lds\n\t" \
  "buffer_load_dwordx4 v0, %0, %8 offen offset:2048 sc1 lds\n\tbuffer_load_dwordx4 v0, %0, %8 offen offset:3072 sc1 lds\n\ts_add_u32 m0, %3, 114688\n\ts_nop 0\n\t" \
  "buffer_load_dwordx4 v0, %1, %9 offen offset:0 sc1 lds\n\tbuffer_load_dwordx4 v0, %1, %9 offen offset:1024 sc1 lds\n\t" \
  "buffer_load_dwordx4 v0, %1, %9 offen offset:2048 sc1 lds\n\tbuffer_load_dwordx4 v0, %1, %9 offen offset:3072 sc1 lds\n\tv_readlane_b32 %8, v5, 20\n\t" \
  "v_readlane_b32 %9, v6, 20\n\ts_waitcnt vmcnt(16) lgkmcnt(0)\n\ts_barrier\n\ts_add_u32 m0, %3, 0\n\ts_nop 0\n\tbuffer_load_dwordx4 v0, %0, %8 offen offset:0 sc1 lds\n\t" \
  "buffer_load_dwordx4 v0, %0, %8 offen offset:1024 sc1 lds\n\tbuffer_load_dwordx4 v0, %0, %8 offen offset:2048 sc1 lds\n\t" \
  "buffer_load_dwordx4 v0, %0, %8 offen offset:3072 sc1 lds\n\ts_add_u32 m0, %3, 65536\n\ts_nop 0\n\tbuffer_load_dwordx4 v0, %1, %9 offen offset:0 sc1 lds\n\t" \
  "buffer_load_dwordx4 v0, %1, %9 offen offset:1024 sc1 lds\n\tbuffer_load_dwordx4 v0, %1, %9 offen offset:2048 sc1 lds\n\t" \
  "buffer_load_dwordx4 v0, %1, %9 offen offset:3072 sc1 lds\n\tv_readlane_b32 %8, v5, 21\n\tv_readlane_b32 %9, v6, 21\n\ts_waitcnt vmcnt(16) lgkmcnt(0)\n\ts_barrier\n\t" \
  "s_add_u32 m0, %3, 16384\n\ts_nop 0\n\tbuffer_load_dwordx4 v0, %0, %8 offen offset:0 sc1 lds\n\tbuffer_load_dwordx4 v0, %0, %8 offen offset:1024 sc1 lds\n\t" \
  "buffer_load_dwordx4 v0, %0, %8 offen offset:2048 sc1 lds\n\tbuffer_load_dwordx4 v0, %0, %8 offen offset:3072 sc1 lds\n\ts_add_u32 m0, %3, 81920\n\ts_nop 0\n\t" \
  "buffer_load_dwordx4 v0, %1, %9 offen offset:0 sc1 lds\n\tbuffer_load_dwordx4 v0, %1, %9 offen offset:1024 sc1 lds\n\t" \
  "buffer_load_dwordx4 v0, %1, %9 offen offset:2048 sc1 lds\n\tbuffer_load_dwordx4 v0, %1, %9 offen offset:3072 sc1 lds\n\tv_readlane_b32 %8, v5, 22\n\t" \
  "v_readlane_b32 %9, v6, 22\n\ts_waitcnt vmcnt(16) lgkmcnt(0)\n\ts_barrier\n\ts_add_u32 m0, %3, 32768\n\ts_nop 0\n\t" \
  "buffer_load_dwordx4 v0, %0, %8 offen offset:0 sc1 lds\n\tbuffer_load_dwordx4 v0, %0, %8 offen offset:1024 sc1 lds\n\t" \
  "buffer_load_dwordx4 v0, %0, %8 offen offset:2048 sc1 lds\n\tbuffer_load_dwordx4 v0, %0, %8 offen offset:3072 sc1 lds\n\ts_add_u32 m0, %3, 98304\n\ts_nop 0\n\t" \
  "buffer_load_dwordx4 v0, %1, %9 offen offset:0 sc1 lds\n\tbuffer_load_dwordx4 v0, %1, %9 offen offset:1024 sc1 lds\n\t" \
  "buffer_load_dwordx4 v0, %1, %9 offen offset:2048 sc1 lds\n\tbuffer_load_dwordx4 v0, %1, %9 offen offset:3072 sc1 lds\n\tv_readlane_b32 %8, v5, 23\n\t" \
  "v_readlane_b32 %9, v6, 23\n\ts_waitcnt vmcnt(16) lgkmcnt(0)\n\ts_barrier\n\ts_add_u32 m0, %3, 49152\n\ts_nop 0\n\t" \
  "buffer_load_dwordx4 v0, %0, %8 offen offset:0 sc1 lds\n\tbuffer_load_dwordx4 v0, %0, %8 offen offset:1024 sc1 lds\n\t" \
  "buffer_load_dwordx4 v0, %0, %8 offen offset:2048 sc1 lds\n\tbuffer_load_dwordx4 v0, %0, %8 offen offset:3072 sc1 lds\n\ts_add_u32 m0, %3, 114688\n\ts_nop 0\n\t" \
  "buffer_load_dwordx4 v0, %1, %9 offen offset:0 sc1 lds\n\tbuffer_load_dwordx4 v0, %1, %9 offen offset:1024 sc1 lds\n\t" \
  "buffer_load_dwordx4 v0, %1, %9 offen offset:2048 sc1 lds\n\tbuffer_load_dwordx4 v0, %1, %9 offen offset:3072 sc1 lds\n\tv_readlane_b32 %8, v5, 24\n\t" \
  "v_readlane_b32 %9, v6, 24\n\ts_waitcnt vmcnt(16) lgkmcnt(0)\n\ts_barrier\n\ts_add_u32 m0, %3, 0\n\ts_nop 0\n\tbuffer_load_dwordx4 v0, %0, %8 offen offset:0 sc1 lds\n\t" \
  "buffer_load_dwordx4 v0, %0, %8 offen offset:1024 sc1 lds\n\tbuffer_load_dwordx4 v0, %0, %8 offen offset:2048 sc1 lds\n\t" \
  "buffer_load_dwordx4 v0, %0, %8 offen offset:3072 sc1 lds\n\ts_add_u32 m0, %3, 65536\n\ts_nop 0\n\tbuffer_load_dwordx4 v0, %1, %9 offen offset:0 sc1 lds\n\t" \
  "buffer_load_dwordx4 v0, %1, %9 offen offset:1024 sc1 lds\n\tbuffer_load_dwordx4 v0, %1, %9 offen offset:2048 sc1 lds\n\t" \
  "buffer_load_dwordx4 v0, %1, %9 offen offset:3072 sc1 lds\n\tv_readlane_b32 %8, v5, 25\n\tv_readlane_b32 %11, v5, 27\n\tv_readlane_b32 %9, v6, 26\n\t" \
  "s_waitcnt vmcnt(16) lgkmcnt(0)\n\ts_barrier\n\ts_add_u32 m0, %3, 16384\n\ts_nop 0\n\tbuffer_load_dwordx4 v0, %0, %8 offen offset:0 sc1 lds\n\t" \
  "buffer_load_dwordx4 v0, %0, %8 offen offset:1024 sc1 lds\n\tbuffer_load_dwordx4 v0, %0, %8 offen offset:2048 sc1 lds\n\t" \
  "buffer_load_dwordx4 v0, %0, %8 offen offset:3072 sc1 lds\n\ts_add_u32 m0, %3, 81920\n\ts_nop 0\n\tbuffer_load_dwordx4 v0, %1, %9 offen offset:0 sc1 lds\n\t" \
  "buffer_load_dwordx4 v0, %1, %9 offen offset:1024 sc1 lds\n\tbuffer_load_dwordx4 v0, %1, %9 offen offset:2048 sc1 lds\n\t" \
  "buffer_load_dwordx4 v0, %1, %9 offen offset:3072 sc1 lds\n\ts_add_u32 m0, %3, 131072\n\ts_nop 0\n\tbuffer_load_dwordx4 v0, %0, %11 offen offset:0 sc1 lds\n\t" \
  "buffer_load_dwordx4 v0, %0, %11 offen offset:1024 sc1 lds\n\tbuffer_load_dwordx4 v0, %0, %11 offen offset:2048 sc1 lds\n\t" \
  "buffer_load_dwordx4 v0, %0, %11 offen offset:3072 sc1 lds\n\tv_readlane_b32 %8, v5, 26\n\tv_readlane_b32 %9, v6, 28\n\ts_waitcnt vmcnt(20) lgkmcnt(0)\n\ts_barrier\n\t" \
  "s_add_u32 m0, %3, 32768\n\ts_nop 0\n\tbuffer_load_dwordx4 v0, %0, %8 offen offset:0 sc1 lds\n\tbuffer_load_dwordx4 v0, %0, %8 offen offset:1024 sc1 lds\n\t" \
  "buffer_load_dwordx4 v0, %0, %8 offen offset:2048 sc1 lds\n\tbuffer_load_dwordx4 v0, %0, %8 offen offset:3072 sc1 lds\n\ts_add_u32 m0, %3, 98304\n\ts_nop 0\n\t" \
  "buffer_load_dwordx4 v0, %1, %9 offen offset:0 sc1 lds\n\tbuffer_load_dwordx4 v0, %1, %9 offen offset:1024 sc1 lds\n\t" \
  "buffer_load_dwordx4 v0, %1, %9 offen offset:2048 sc1 lds\n\tbuffer_load_dwordx4 v0, %1, %9 offen offset:3072 sc1 lds\n\tv_readlane_b32 %8, v5, 28\n\t" \
  "v_readlane_b32 %9, v6, 30\n\ts_waitcnt vmcnt(20) lgkmcnt(0)\n\ts_barrier\n\ts_add_u32 m0, %3, 49152\n\ts_nop 0\n\t" \
  "buffer_load_dwordx4 v0, %0, %8 offen offset:0 sc1 lds\n\tbuffer_load_dwordx4 v0, %0, %8 offen offset:1024 sc1 lds\n\t" \
  "buffer_load_dwordx4 v0, %0, %8 offen offset:2048 sc1 lds\n\tbuffer_load_dwordx4 v0, %0, %8 offen offset:3072 sc1 lds\n\ts_add_u32 m0, %3, 114688\n\ts_nop 0\n\t" \
  "buffer_load_dwordx4 v0, %1, %9 offen offset:0 sc1 lds\n\tbuffer_load_dwordx4 v0, %1, %9 offen offset:1024 sc1 lds\n\t" \
  "buffer_load_dwordx4 v0, %1, %9 offen offset:2048 sc1 lds\n\tbuffer_load_dwordx4 v0, %1, %9 offen offset:3072 sc1 lds\n\tv_readlane_b32 %8, v5, 29\n\t" \
  "s_waitcnt vmcnt(24) lgkmcnt(0)\n\ts_barrier\n\ts_waitcnt vmcnt(12) lgkmcnt(0)\n\ts_barrier\n\ts_add_u32 m0, %3, 0\n\ts_nop 0\n\t" \
  "buffer_load_dwordx4 v0, %0, %8 offen offset:0 sc1 lds\n\tbuffer_load_dwordx4 v0, %0, %8 offen offset:1024 sc1 lds\n\t" \
  "buffer_load_dwordx4 v0, %0, %8 offen offset:2048 sc1 lds\n\tbuffer_load_dwordx4 v0, %0, %8 offen offset:3072 sc1 lds\n\tv_readlane_b32 %8, v5, 30\n\ts_nop 1\n\t" \
  "s_waitcnt lgkmcnt(0)\n\ts_barrier\n\ts_add_u32 m0, %3, 16384\n\ts_nop 0\n\tbuffer_load_dwordx4 v0, %0, %8 offen offset:0 sc1 lds\n\t" \
  "buffer_load_dwordx4 v0, %0, %8 offen offset:1024 sc1 lds\n\tbuffer_load_dwordx4 v0, %0, %8 offen offset:2048 sc1 lds\n\t" \
  "buffer_load_dwordx4 v0, %0, %8 offen offset:3072 sc1 lds\n\tv_readlane_b32 %8, v5, 31\n\ts_nop 1\n\ts_waitcnt vmcnt(12) lgkmcnt(0)\n\ts_barrier\n\t" \
  "s_add_u32 m0, %3, 32768\n\ts_nop 0\n\tbuffer_load_dwordx4 v0, %0, %8 offen offset:0 sc1 lds\n\tbuffer_load_dwordx4 v0, %0, %8 offen offset:1024 sc1 lds\n\t" \
  "buffer_load_dwordx4 v0, %0, %8 offen offset:2048 sc1 lds\n\tbuffer_load_dwordx4 v0, %0, %8 offen offset:3072 sc1 lds\n\ts_waitcnt vmcnt(8) lgkmcnt(0)\n\ts_barrier\n\t" \
  "s_waitcnt vmcnt(4) lgkmcnt(0)\n\ts_barrier\n\ts_waitcnt vmcnt(0) lgkmcnt(0)\n\ts_barrier\n\ts_waitcnt lgkmcnt(0)\n\ts_barrier\n\ts_branch .Lke%=\n\t.Lks%=:\n\t" \
  "s_mov_b32 m0, 0x20000\n\t.Lkt%=:\n\tbuffer_load_dwordx2 v[16:17], v7, %2, 0 offen sc1\n\ts_waitcnt vmcnt(0)\n\tv_cmp_eq_u64 vcc, %5, v[16:17]\n\t" \
  "s_lshr_b32 %6, vcc_lo, 8\n\ts_and_b32 %6, %6, vcc_lo\n\ts_bitcmp1_b32 %6, %11\n\ts_cbranch_scc1 .Lku%=\n\ts_sleep 4\n\ts_sub_u32 m0, m0, 1\n\ts_cbranch_scc0 .Lkt%=\n\t" \
  ".Lku%=:\n\ts_setpc_b64 %7\n\t.Lkm%=:\n\ts_sleep 4\n\tbuffer_load_dwordx2 v[18:19], v8, %2, 0 offen sc1\n\ts_waitcnt vmcnt(0)\n\ts_sub_u32 %11, %11, 1\n\t" \
  "s_cbranch_scc0 .Lkl%=\n\ts_branch .Lkn%=\n\t.Lke%=:\n\t"
#define FFN2_LCLOB \
  "v16", "v17", "v18", "v19"
#define OPROJ_PRE \
  "s_nop 4\n\ts_and_b32 %0, %[m], %[rem]\n\ts_cbranch_scc0 .Lon%=\n\ts_and_b32 %[t], %0, %[hm]\n\ts_cselect_b32 %0, %[t], %0\n\ts_ff1_i32_b32 %[c0], %0\n\t" \
  "s_bitset0_b32 %[rem], %[c0]\n\ts_lshl_b32 %0, %[c0], 14\n\ts_xor_b32 %0, %0, %[x]\n\tbuffer_load_dwordx4 v[144:147], %1, %[rx], %0 offen offset:0 sc1\n\t" \
  "buffer_load_dwordx4 v[148:151], %1, %[rx], %0 offen offset:2048 sc1\n\ts_waitcnt vmcnt(2)\n\ts_barrier\n\t" \
  "buffer_load_dwordx4 v[152:155], %1, %[rx], %0 offen offset:1024 sc1\n\tbuffer_load_dwordx4 v[156:159], %1, %[rx], %0 offen offset:3072 sc1\n\ts_xor_b32 %0, %0, 0x4000\n\t" \
  "buffer_load_dwordx4 v[160:163], %1, %[rx], %0 offen offset:0 sc1\n\tbuffer_load_dwordx4 v[164:167], %1, %[rx], %0 offen offset:2048 sc1\n\t" \
  "buffer_load_dwordx4 v[168:171], %1, %[rx], %0 offen offset:1024 sc1\n\tbuffer_load_dwordx4 v[172:175], %1, %[rx], %0 offen offset:3072 sc1\n\ts_xor_b32 %0, %0, 0xc000\n\t" \
  "buffer_load_dwordx4 v[176:179], %1, %[rx], %0 offen offset:0 sc1\n\tbuffer_load_dwordx4 v[180:183], %1, %[rx], %0 offen offset:2048 sc1\n\t" \
  "buffer_load_dwordx4 v[184:187], %1, %[rx], %0 offen offset:1024 sc1\n\tbuffer_load_dwordx4 v[188:191], %1, %[rx], %0 offen offset:3072 sc1\n\ts_xor_b32 %0, %0, 0x4000\n\t" \
  "buffer_load_dwordx4 v[192:195], %1, %[rx], %0 offen offset:0 sc1\n\tbuffer_load_dwordx4 v[196:199], %1, %[rx], %0 offen offset:2048 sc1\n\t" \
  "buffer_load_dwordx4 v[200:203], %1, %[rx], %0 offen offset:1024 sc1\n\tbuffer_load_dwordx4 v[204:207], %1, %[rx], %0 offen offset:3072 sc1\n\t" \
  "s_mul_i32 %[t], %[c0], 8448\n\tv_add_u32 %2, %[t], %2\n\tds_read_b64_tr_b16 v[96:97], %2 offset:0\n\tds_read_b64_tr_b16 v[98:99], %2 offset:512\n\t" \
  "ds_read_b64_tr_b16 v[100:101], %2 offset:32\n\tds_read_b64_tr_b16 v[102:103], %2 offset:544\n\tds_read_b64_tr_b16 v[104:105], %2 offset:256\n\t" \
  "ds_read_b64_tr_b16 v[106:107], %2 offset:768\n\tds_read_b64_tr_b16 v[108:109], %2 offset:288\n\tds_read_b64_tr_b16 v[110:111], %2 offset:800\n\t" \
  "ds_read_b64_tr_b16 v[112:113], %2 offset:4224\n\tds_read_b64_tr_b16 v[114:115], %2 offset:4736\n\tds_read_b64_tr_b16 v[116:117], %2 offset:4256\n\t" \
  "ds_read_b64_tr_b16 v[118:119], %2 offset:4768\n\tds_read_b64_tr_b16 v[120:121], %2 offset:4480\n\tds_read_b64_tr_b16 v[122:123], %2 offset:4992\n\t" \
  "ds_read_b64_tr_b16 v[124:125], %2 offset:4512\n\tds_read_b64_tr_b16 v[126:127], %2 offset:5024\n\t.Lon%=:\n\t"
#define OPROJ_SEL "{v[224:231]}"
#define OPROJ_LOOP \
  "s_cmp_eq_u32 %[rem], 0x111\n\ts_cbranch_scc1 .Lon%=\n\t.Log%=:\n\t.Lop1%=:\n\ts_and_b32 %0, %[m], %[rem]\n\ts_cbranch_scc0 .Los1%=\n\ts_and_b32 %[t], %0, %[hm]\n\t" \
  "s_cselect_b32 %0, %[t], %0\n\ts_ff1_i32_b32 %[c1], %0\n\ts_bitset0_b32 %[rem], %[c1]\n\ts_lshl_b32 %0, %[c1], 14\n\ts_xor_b32 %0, %0, %[x]\n\t" \
  "s_waitcnt vmcnt(14) lgkmcnt(8)\n\tv_mfma_f32_16x16x32_bf16 v[0:3], v[96:99], v[144:147], 0\n\tds_read_b64_tr_b16 v[128:129], %2 offset:8448\n\t" \
  "v_mfma_f32_16x16x32_bf16 v[4:7], v[96:99], v[148:151], 0\n\tds_read_b64_tr_b16 v[130:131], %2 offset:8960\n\t" \
  "buffer_load_dwordx4 v[208:211], %1, %[rx], %0 offen offset:0 sc1\n\tv_mfma_f32_16x16x32_bf16 v[8:11], v[100:103], v[144:147], 0\n\t" \
  "ds_read_b64_tr_b16 v[132:133], %2 offset:8480\n\ts_sub_u32 %[t], %[c1], %[c0]\n\tv_mfma_f32_16x16x32_bf16 v[12:15], v[100:103], v[148:151], 0\n\t" \
  "ds_read_b64_tr_b16 v[134:135], %2 offset:8992\n\ts_mul_i32 %[t], %[t], 8448\n\tv_mfma_f32_16x16x32_bf16 v[16:19], v[104:107], v[144:147], 0\n\t" \
  "ds_read_b64_tr_b16 v[136:137], %2 offset:8704\n\tbuffer_load_dwordx4 v[212:215], %1, %[rx], %0 offen offset:2048 sc1\n\t" \
  "v_mfma_f32_16x16x32_bf16 v[20:23], v[104:107], v[148:151], 0\n\tds_read_b64_tr_b16 v[138:139], %2 offset:9216\n\tv_add_u32 %3, %[t], %2\n\t" \
  "v_mfma_f32_16x16x32_bf16 v[24:27], v[108:111], v[144:147], 0\n\tds_read_b64_tr_b16 v[140:141], %2 offset:8736\n\t" \
  "v_mfma_f32_16x16x32_bf16 v[28:31], v[108:111], v[148:151], 0\n\tds_read_b64_tr_b16 v[142:143], %2 offset:9248\n\ts_waitcnt vmcnt(14) lgkmcnt(8)\n\t" \
  "v_mfma_f32_16x16x32_bf16 v[0:3], v[112:115], v[152:155], v[0:3]\n\tds_read_b64_tr_b16 v[96:97], %2 offset:12672\n\t" \
  "v_mfma_f32_16x16x32_bf16 v[4:7], v[112:115], v[156:159], v[4:7]\n\tds_read_b64_tr_b16 v[98:99], %2 offset:13184\n\t" \
  "buffer_load_dwordx4 v[216:219], %1, %[rx], %0 offen offset:1024 sc1\n\tv_mfma_f32_16x16x32_bf16 v[8:11], v[116:119], v[152:155], v[8:11]\n\t" \
  "ds_read_b64_tr_b16 v[100:101], %2 offset:12704\n\tv_mfma_f32_16x16x32_bf16 v[12:15], v[116:119], v[156:159], v[12:15]\n\t" \
  "ds_read_b64_tr_b16 v[102:103], %2 offset:13216\n\tv_mfma_f32_16x16x32_bf16 v[16:19], v[120:123], v[152:155], v[16:19]\n\t" \
  "ds_read_b64_tr_b16 v[104:105], %2 offset:12928\n\tbuffer_load_dwordx4 v[220:223], %1, %[rx], %0 offen offset:3072 sc1\n\t" \
  "v_mfma_f32_16x16x32_bf16 v[20:23], v[120:123], v[156:159], v[20:23]\n\tds_read_b64_tr_b16 v[106:107], %2 offset:13440\n\ts_xor_b32 %0, %0, 0x4000\n\t" \
  "v_mfma_f32_16x16x32_bf16 v[24:27], v[124:127], v[152:155], v[24:27]\n\tds_read_b64_tr_b16 v[108:109], %2 offset:12960\n\t" \
  "v_mfma_f32_16x16x32_bf16 v[28:31], v[124:127], v[156:159], v[28:31]\n\tds_read_b64_tr_b16 v[110:111], %2 offset:13472\n\ts_waitcnt vmcnt(14) lgkmcnt(8)\n\t" \
  "v_mfma_f32_16x16x32_bf16 v[0:3], v[128:131], v[160:163], v[0:3]\n\tds_read_b64_tr_b16 v[112:113], %2 offset:16896\n\t" \
  "v_mfma_f32_16x16x32_bf16 v[4:7], v[128:131], v[164:167], v[4:7]\n\tds_read_b64_tr_b16 v[114:115], %2 offset:17408\n\t" \
  "buffer_load_dwordx4 v[144:147], %1, %[rx], %0 offen offset:0 sc1\n\tv_mfma_f32_16x16x32_bf16 v[8:11], v[132:135], v[160:163], v[8:11]\n\t" \
  "ds_read_b64_tr_b16 v[116:117], %2 offset:16928\n\tv_mfma_f32_16x16x32_bf16 v[12:15], v[132:135], v[164:167], v[12:15]\n\t" \
  "ds_read_b64_tr_b16 v[118:119], %2 offset:17440\n\tv_mfma_f32_16x16x32_bf16 v[16:19], v[136:139], v[160:163], v[16:19]\n\t" \
  "ds_read_b64_tr_b16 v[120:121], %2 offset:17152\n\tbuffer_load_dwordx4 v[148:151], %1, %[rx], %0 offen offset:2048 sc1\n\t" \
  "v_mfma_f32_16x16x32_bf16 v[20:23], v[136:139], v[164:167], v[20:23]\n\tds_read_b64_tr_b16 v[122:123], %2 offset:17664\n\t" \
  "v_mfma_f32_16x16x32_bf16 v[24:27], v[140:143], v[160:163], v[24:27]\n\tds_read_b64_tr_b16 v[124:125], %2 offset:17184\n\t" \
  "v_mfma_f32_16x16x32_bf16 v[28:31], v[140:143], v[164:167], v[28:31]\n\tds_read_b64_tr_b16 v[126:127], %2 offset:17696\n\ts_waitcnt vmcnt(14) lgkmcnt(8)\n\t" \
  "v_mfma_f32_16x16x32_bf16 v[0:3], v[96:99], v[168:171], v[0:3]\n\tds_read_b64_tr_b16 v[128:129], %2 offset:21120\n\t" \
  "v_mfma_f32_16x16x32_bf16 v[4:7], v[96:99], v[172:175], v[4:7]\n\tds_read_b64_tr_b16 v[130:131], %2 offset:21632\n\t" \
  "buffer_load_dwordx4 v[152:155], %1, %[rx], %0 offen offset:1024 sc1\n\tv_mfma_f32_16x16x32_bf16 v[8:11], v[100:103], v[168:171], v[8:11]\n\t" \
  "ds_read_b64_tr_b16 v[132:133], %2 offset:21152\n\tv_mfma_f32_16x16x32_bf16 v[12:15], v[100:103], v[172:175], v[12:15]\n\t" \
  "ds_read_b64_tr_b16 v[134:135], %2 offset:21664\n\tv_mfma_f32_16x16x32_bf16 v[16:19], v[104:107], v[168:171], v[16:19]\n\t" \
  "ds_read_b64_tr_b16 v[136:137], %2 offset:21376\n\tbuffer_load_dwordx4 v[156:159], %1, %[rx], %0 offen offset:3072 sc1\n\t" \
  "v_mfma_f32_16x16x32_bf16 v[20:23], v[104:107], v[172:175], v[20:23]\n\tds_read_b64_tr_b16 v[138:139], %2 offset:21888\n\ts_xor_b32 %0, %0, 0xc000\n\t" \
  "v_mfma_f32_16x16x32_bf16 v[24:27], v[108:111], v[168:171], v[24:27]\n\tds_read_b64_tr_b16 v[140:141], %2 offset:21408\n\t" \
  "v_mfma_f32_16x16x32_bf16 v[28:31], v[108:111], v[172:175], v[28:31]\n\tds_read_b64_tr_b16 v[142:143], %2 offset:21920\n\ts_waitcnt vmcnt(14) lgkmcnt(8)\n\t" \
  "v_mfma_f32_16x16x32_bf16 v[0:3], v[112:115], v[176:179], v[0:3]\n\tds_read_b64_tr_b16 v[96:97], %2 offset:25344\n\t" \
  "v_mfma_f32_16x16x32_bf16 v[4:7], v[112:115], v[180:183], v[4:7]\n\tds_read_b64_tr_b16 v[98:99], %2 offset:25856\n\t" \
  "buffer_load_dwordx4 v[160:163], %1, %[rx], %0 offen offset:0 sc1\n\tv_mfma_f32_16x16x32_bf16 v[8:11], v[116:119], v[176:179], v[8:11]\n\t" \
  "ds_read_b64_tr_b16 v[100:101], %2 offset:25376\n\tv_mfma_f32_16x16x32_bf16 v[12:15], v[116:119], v[180:183], v[12:15]\n\t" \
  "ds_read_b64_tr_b16 v[102:103], %2 offset:25888\n\tv_mfma_f32_16x16x32_bf16 v[16:19], v[120:123], v[176:179], v[16:19]\n\t" \
  "ds_read_b64_tr_b16 v[104:105], %2 offset:25600\n\tbuffer_load_dwordx4 v[164:167], %1, %[rx], %0 offen offset:2048 sc1\n\t" \
  "v_mfma_f32_16x16x32_bf16 v[20:23], v[120:123], v[180:183], v[20:23]\n\tds_read_b64_tr_b16 v[106:107], %2 offset:26112\n\t" \
  "v_mfma_f32_16x16x32_bf16 v[24:27], v[124:127], v[176:179], v[24:27]\n\tds_read_b64_tr_b16 v[108:109], %2 offset:25632\n\t" \
  "v_mfma_f32_16x16x32_bf16 v[28:31], v[124:127], v[180:183], v[28:31]\n\tds_read_b64_tr_b16 v[110:111], %2 offset:26144\n\ts_waitcnt vmcnt(14) lgkmcnt(8)\n\t" \
  "v_mfma_f32_16x16x32_bf16 v[0:3], v[128:131], v[184:187], v[0:3]\n\tds_read_b64_tr_b16 v[112:113], %2 offset:29568\n\t" \
  "v_mfma_f32_16x16x32_bf16 v[4:7], v[128:131], v[188:191], v[4:7]\n\tds_read_b64_tr_b16 v[114:115], %2 offset:30080\n\t" \
  "buffer_load_dwordx4 v[168:171], %1, %[rx], %0 offen offset:1024 sc1\n\tv_mfma_f32_16x16x32_bf16 v[8:11], v[132:135], v[184:187], v[8:11]\n\t" \
  "ds_read_b64_tr_b16 v[116:117], %2 offset:29600\n\tv_mfma_f32_16x16x32_bf16 v[12:15], v[132:135], v[188:191], v[12:15]\n\t" \
  "ds_read_b64_tr_b16 v[118:119], %2 offset:30112\n\tv_mfma_f32_16x16x32_bf16 v[16:19], v[136:139], v[184:187], v[16:19]\n\t" \
  "ds_read_b64_tr_b16 v[120:121], %2 offset:29824\n\tbuffer_load_dwordx4 v[172:175], %1, %[rx], %0 offen offset:3072 sc1\n\t" \
  "v_mfma_f32_16x16x32_bf16 v[20:23], v[136:139], v[188:191], v[20:23]\n\tds_read_b64_tr_b16 v[122:123], %2 offset:30336\n\ts_xor_b32 %0, %0, 0x4000\n\t" \
  "v_mfma_f32_16x16x32_bf16 v[24:27], v[140:143], v[184:187], v[24:27]\n\tds_read_b64_tr_b16 v[124:125], %2 offset:29856\n\t" \
  "v_mfma_f32_16x16x32_bf16 v[28:31], v[140:143], v[188:191], v[28:31]\n\tds_read_b64_tr_b16 v[126:127], %2 offset:30368\n\ts_waitcnt vmcnt(14) lgkmcnt(8)\n\t" \
  "v_mfma_f32_16x16x32_bf16 v[0:3], v[96:99], v[192:195], v[0:3]\n\tds_read_b64_tr_b16 v[128:129], %3 offset:0\n\t" \
  "v_mfma_f32_16x16x32_bf16 v[4:7], v[96:99], v[196:199], v[4:7]\n\tds_read_b64_tr_b16 v[130:131], %3 offset:512\n\t" \
  "buffer_load_dwordx4 v[176:179], %1, %[rx], %0 offen offset:0 sc1\n\tv_mfma_f32_16x16x32_bf16 v[8:11], v[100:103], v[192:195], v[8:11]\n\t" \
  "ds_read_b64_tr_b16 v[132:133], %3 offset:32\n\tv_mfma_f32_16x16x32_bf16 v[12:15], v[100:103], v[196:199], v[12:15]\n\tds_read_b64_tr_b16 v[134:135], %3 offset:544\n\t" \
  "v_mfma_f32_16x16x32_bf16 v[16:19], v[104:107], v[192:195], v[16:19]\n\tds_read_b64_tr_b16 v[136:137], %3 offset:256\n\t" \
  "buffer_load_dwordx4 v[180:183], %1, %[rx], %0 offen offset:2048 sc1\n\tv_mfma_f32_16x16x32_bf16 v[20:23], v[104:107], v[196:199], v[20:23]\n\t" \
  "ds_read_b64_tr_b16 v[138:139], %3 offset:768\n\tv_mfma_f32_16x16x32_bf16 v[24:27], v[108:111], v[192:195], v[24:27]\n\tds_read_b64_tr_b16 v[140:141], %3 offset:288\n\t" \
  "v_mfma_f32_16x16x32_bf16 v[28:31], v[108:111], v[196:199], v[28:31]\n\tds_read_b64_tr_b16 v[142:143], %3 offset:800\n\ts_waitcnt vmcnt(14) lgkmcnt(8)\n\t" \
  "v_mfma_f32_16x16x32_bf16 v[0:3], v[112:115], v[200:203], v[0:3]\n\tds_read_b64_tr_b16 v[96:97], %3 offset:4224\n\t" \
  "v_mfma_f32_16x16x32_bf16 v[4:7], v[112:115], v[204:207], v[4:7]\n\tds_read_b64_tr_b16 v[98:99], %3 offset:4736\n\t" \
  "buffer_load_dwordx4 v[184:187], %1, %[rx], %0 offen offset:1024 sc1\n\tv_mfma_f32_16x16x32_bf16 v[8:11], v[116:119], v[200:203], v[8:11]\n\t" \
  "ds_read_b64_tr_b16 v[100:101], %3 offset:4256\n\tv_mfma_f32_16x16x32_bf16 v[12:15], v[116:119], v[204:207], v[12:15]\n\tds_read_b64_tr_b16 v[102:103], %3 offset:4768\n\t" \
  "v_mfma_f32_16x16x32_bf16 v[16:19], v[120:123], v[200:203], v[16:19]\n\tds_read_b64_tr_b16 v[104:105], %3 offset:4480\n\t" \
  "buffer_load_dwordx4 v[188:191], %1, %[rx], %0 offen offset:3072 sc1\n\tv_mfma_f32_16x16x32_bf16 v[20:23], v[120:123], v[204:207], v[20:23]\n\t" \
  "ds_read_b64_tr_b16 v[106:107], %3 offset:4992\n\tv_mfma_f32_16x16x32_bf16 v[24:27], v[124:127], v[200:203], v[24:27]\n\tds_read_b64_tr_b16 v[108:109], %3 offset:4512\n\t" \
  "v_mfma_f32_16x16x32_bf16 v[28:31], v[124:127], v[204:207], v[28:31]\n\tds_read_b64_tr_b16 v[110:111], %3 offset:5024\n\t.Lop2%=:\n\ts_and_b32 %0, %[m], %[rem]\n\t" \
  "s_cbranch_scc0 .Los2%=\n\ts_and_b32 %[t], %0, %[hm]\n\ts_cselect_b32 %0, %[t], %0\n\ts_ff1_i32_b32 %[c2], %0\n\ts_bitset0_b32 %[rem], %[c2]\n\t" \
  "s_lshl_b32 %0, %[c2], 14\n\ts_xor_b32 %0, %0, %[x]\n\ts_waitcnt vmcnt(14) lgkmcnt(8)\n\tv_mfma_f32_16x16x32_bf16 v[32:35], v[128:131], v[208:211], 0\n\t" \
  "ds_read_b64_tr_b16 v[112:113], %3 offset:8448\n\tv_mfma_f32_16x16x32_bf16 v[36:39], v[128:131], v[212:215], 0\n\tds_read_b64_tr_b16 v[114:115], %3 offset:8960\n\t" \
  "buffer_load_dwordx4 v[192:195], %1, %[rx], %0 offen offset:0 sc1\n\tv_mfma_f32_16x16x32_bf16 v[40:43], v[132:135], v[208:211], 0\n\t" \
  "ds_read_b64_tr_b16 v[116:117], %3 offset:8480\n\ts_sub_u32 %[t], %[c2], %[c1]\n\tv_mfma_f32_16x16x32_bf16 v[44:47], v[132:135], v[212:215], 0\n\t" \
  "ds_read_b64_tr_b16 v[118:119], %3 offset:8992\n\ts_mul_i32 %[t], %[t], 8448\n\tv_mfma_f32_16x16x32_bf16 v[48:51], v[136:139], v[208:211], 0\n\t" \
  "ds_read_b64_tr_b16 v[120:121], %3 offset:8704\n\tbuffer_load_dwordx4 v[196:199], %1, %[rx], %0 offen offset:2048 sc1\n\t" \
  "v_mfma_f32_16x16x32_bf16 v[52:55], v[136:139], v[212:215], 0\n\tds_read_b64_tr_b16 v[122:123], %3 offset:9216\n\tv_add_u32 %2, %[t], %3\n\t" \
  "v_mfma_f32_16x16x32_bf16 v[56:59], v[140:143], v[208:211], 0\n\tds_read_b64_tr_b16 v[124:125], %3 offset:8736\n\t" \
  "v_mfma_f32_16x16x32_bf16 v[60:63], v[140:143], v[212:215], 0\n\tds_read_b64_tr_b16 v[126:127], %3 offset:9248\n\ts_waitcnt vmcnt(14) lgkmcnt(8)\n\t" \
  "v_mfma_f32_16x16x32_bf16 v[32:35], v[96:99], v[216:219], v[32:35]\n\tds_read_b64_tr_b16 v[128:129], %3 offset:12672\n\t" \
  "v_mfma_f32_16x16x32_bf16 v[36:39], v[96:99], v[220:223], v[36:39]\n\tds_read_b64_tr_b16 v[130:131], %3 offset:13184\n\t" \
  "buffer_load_dwordx4 v[200:203], %1, %[rx], %0 offen offset:1024 sc1\n\tv_mfma_f32_16x16x32_bf16 v[40:43], v[100:103], v[216:219], v[40:43]\n\t" \
  "ds_read_b64_tr_b16 v[132:133], %3 offset:12704\n\tv_mfma_f32_16x16x32_bf16 v[44:47], v[100:103], v[220:223], v[44:47]\n\t" \
  "ds_read_b64_tr_b16 v[134:135], %3 offset:13216\n\tv_mfma_f32_16x16x32_bf16 v[48:51], v[104:107], v[216:219], v[48:51]\n\t" \
  "ds_read_b64_tr_b16 v[136:137], %3 offset:12928\n\tbuffer_load_dwordx4 v[204:207], %1, %[rx], %0 offen offset:3072 sc1\n\t" \
  "v_mfma_f32_16x16x32_bf16 v[52:55], v[104:107], v[220:223], v[52:55]\n\tds_read_b64_tr_b16 v[138:139], %3 offset:13440\n\ts_xor_b32 %0, %0, 0x4000\n\t" \
  "v_mfma_f32_16x16x32_bf16 v[56:59], v[108:111], v[216:219], v[56:59]\n\tds_read_b64_tr_b16 v[140:141], %3 offset:12960\n\t" \
  "v_mfma_f32_16x16x32_bf16 v[60:63], v[108:111], v[220:223], v[60:63]\n\tds_read_b64_tr_b16 v[142:143], %3 offset:13472\n\ts_waitcnt vmcnt(14) lgkmcnt(8)\n\t" \
  "v_mfma_f32_16x16x32_bf16 v[32:35], v[112:115], v[144:147], v[32:35]\n\tds_read_b64_tr_b16 v[96:97], %3 offset:16896\n\t" \
  "v_mfma_f32_16x16x32_bf16 v[36:39], v[112:115], v[148:151], v[36:39]\n\tds_read_b64_tr_b16 v[98:99], %3 offset:17408\n\t" \
  "buffer_load_dwordx4 v[208:211], %1, %[rx], %0 offen offset:0 sc1\n\tv_mfma_f32_16x16x32_bf16 v[40:43], v[116:119], v[144:147], v[40:43]\n\t" \
  "ds_read_b64_tr_b16 v[100:101], %3 offset:16928\n\tv_mfma_f32_16x16x32_bf16 v[44:47], v[116:119], v[148:151], v[44:47]\n\t" \
  "ds_read_b64_tr_b16 v[102:103], %3 offset:17440\n\tv_mfma_f32_16x16x32_bf16 v[48:51], v[120:123], v[144:147], v[48:51]\n\t" \
  "ds_read_b64_tr_b16 v[104:105], %3 offset:17152\n\tbuffer_load_dwordx4 v[212:215], %1, %[rx], %0 offen offset:2048 sc1\n\t" \
  "v_mfma_f32_16x16x32_bf16 v[52:55], v[120:123], v[148:151], v[52:55]\n\tds_read_b64_tr_b16 v[106:107], %3 offset:17664\n\t" \
  "v_mfma_f32_16x16x32_bf16 v[56:59], v[124:127], v[144:147], v[56:59]\n\tds_read_b64_tr_b16 v[108:109], %3 offset:17184\n\t" \
  "v_mfma_f32_16x16x32_bf16 v[60:63], v[124:127], v[148:151], v[60:63]\n\tds_read_b64_tr_b16 v[110:111], %3 offset:17696\n\ts_waitcnt vmcnt(14) lgkmcnt(8)\n\t" \
  "v_mfma_f32_16x16x32_bf16 v[32:35], v[128:131], v[152:155], v[32:35]\n\tds_read_b64_tr_b16 v[112:113], %3 offset:21120\n\t" \
  "v_mfma_f32_16x16x32_bf16 v[36:39], v[128:131], v[156:159], v[36:39]\n\tds_read_b64_tr_b16 v[114:115], %3 offset:21632\n\t" \
  "buffer_load_dwordx4 v[216:219], %1, %[rx], %0 offen offset:1024 sc1\n\tv_mfma_f32_16x16x32_bf16 v[40:43], v[132:135], v[152:155], v[40:43]\n\t" \
  "ds_read_b64_tr_b16 v[116:117], %3 offset:21152\n\tv_mfma_f32_16x16x32_bf16 v[44:47], v[132:135], v[156:159], v[44:47]\n\t" \
  "ds_read_b64_tr_b16 v[118:119], %3 offset:21664\n\tv_mfma_f32_16x16x32_bf16 v[48:51], v[136:139], v[152:155], v[48:51]\n\t" \
  "ds_read_b64_tr_b16 v[120:121], %3 offset:21376\n\tbuffer_load_dwordx4 v[220:223], %1, %[rx], %0 offen offset:3072 sc1\n\t" \
  "v_mfma_f32_16x16x32_bf16 v[52:55], v[136:139], v[156:159], v[52:55]\n\tds_read_b64_tr_b16 v[122:123], %3 offset:21888\n\ts_xor_b32 %0, %0, 0xc000\n\t" \
  "v_mfma_f32_16x16x32_bf16 v[56:59], v[140:143], v[152:155], v[56:59]\n\tds_read_b64_tr_b16 v[124:125], %3 offset:21408\n\t" \
  "v_mfma_f32_16x16x32_bf16 v[60:63], v[140:143], v[156:159], v[60:63]\n\tds_read_b64_tr_b16 v[126:127], %3 offset:21920\n\ts_waitcnt vmcnt(14) lgkmcnt(8)\n\t" \
  "v_mfma_f32_16x16x32_bf16 v[32:35], v[96:99], v[160:163], v[32:35]\n\tds_read_b64_tr_b16 v[128:129], %3 offset:25344\n\t" \
  "v_mfma_f32_16x16x32_bf16 v[36:39], v[96:99], v[164:167], v[36:39]\n\tds_read_b64_tr_b16 v[130:131], %3 offset:25856\n\t" \
  "buffer_load_dwordx4 v[144:147], %1, %[rx], %0 offen offset:0 sc1\n\tv_mfma_f32_16x16x32_bf16 v[40:43], v[100:103], v[160:163], v[40:43]\n\t" \
  "ds_read_b64_tr_b16 v[132:133], %3 offset:25376\n\tv_mfma_f32_16x16x32_bf16 v[44:47], v[100:103], v[164:167], v[44:47]\n\t" \
  "ds_read_b64_tr_b16 v[134:135], %3 offset:25888\n\tv_mfma_f32_16x16x32_bf16 v[48:51], v[104:107], v[160:163], v[48:51]\n\t" \
  "ds_read_b64_tr_b16 v[136:137], %3 offset:25600\n\tbuffer_load_dwordx4 v[148:151], %1, %[rx], %0 offen offset:2048 sc1\n\t" \
  "v_mfma_f32_16x16x32_bf16 v[52:55], v[104:107], v[164:167], v[52:55]\n\tds_read_b64_tr_b16 v[138:139], %3 offset:26112\n\t" \
  "v_mfma_f32_16x16x32_bf16 v[56:59], v[108:111], v[160:163], v[56:59]\n\tds_read_b64_tr_b16 v[140:141], %3 offset:25632\n\t" \
  "v_mfma_f32_16x16x32_bf16 v[60:63], v[108:111], v[164:167], v[60:63]\n\tds_read_b64_tr_b16 v[142:143], %3 offset:26144\n\ts_waitcnt vmcnt(14) lgkmcnt(8)\n\t" \
  "v_mfma_f32_16x16x32_bf16 v[32:35], v[112:115], v[168:171], v[32:35]\n\tds_read_b64_tr_b16 v[96:97], %3 offset:29568\n\t" \
  "v_mfma_f32_16x16x32_bf16 v[36:39], v[112:115], v[172:175], v[36:39]\n\tds_read_b64_tr_b16 v[98:99], %3 offset:30080\n\t" \
  "buffer_load_dwordx4 v[152:155], %1, %[rx], %0 offen offset:1024 sc1\n\tv_mfma_f32_16x16x32_bf16 v[40:43], v[116:119], v[168:171], v[40:43]\n\t" \
  "ds_read_b64_tr_b16 v[100:101], %3 offset:29600\n\tv_mfma_f32_16x16x32_bf16 v[44:47], v[116:119], v[172:175], v[44:47]\n\t" \
  "ds_read_b64_tr_b16 v[102:103], %3 offset:30112\n\tv_mfma_f32_16x16x32_bf16 v[48:51], v[120:123], v[168:171], v[48:51]\n\t" \
  "ds_read_b64_tr_b16 v[104:105], %3 offset:29824\n\tbuffer_load_dwordx4 v[156:159], %1, %[rx], %0 offen offset:3072 sc1\n\t" \
  "v_mfma_f32_16x16x32_bf16 v[52:55], v[120:123], v[172:175], v[52:55]\n\tds_read_b64_tr_b16 v[106:107], %3 offset:30336\n\ts_xor_b32 %0, %0, 0x4000\n\t" \
  "v_mfma_f32_16x16x32_bf16 v[56:59], v[124:127], v[168:171], v[56:59]\n\tds_read_b64_tr_b16 v[108:109], %3 offset:29856\n\t" \
  "v_mfma_f32_16x16x32_bf16 v[60:63], v[124:127], v[172:175], v[60:63]\n\tds_read_b64_tr_b16 v[110:111], %3 offset:30368\n\ts_waitcnt vmcnt(14) lgkmcnt(8)\n\t" \
  "v_mfma_f32_16x16x32_bf16 v[32:35], v[128:131], v[176:179], v[32:35]\n\tds_read_b64_tr_b16 v[112:113], %2 offset:0\n\t" \
  "v_mfma_f32_16x16x32_bf16 v[36:39], v[128:131], v[180:183], v[36:39]\n\tds_read_b64_tr_b16 v[114:115], %2 offset:512\n\t" \
  "buffer_load_dwordx4 v[160:163], %1, %[rx], %0 offen offset:0 sc1\n\tv_mfma_f32_16x16x32_bf16 v[40:43], v[132:135], v[176:179], v[40:43]\n\t" \
  "ds_read_b64_tr_b16 v[116:117], %2 offset:32\n\tv_mfma_f32_16x16x32_bf16 v[44:47], v[132:135], v[180:183], v[44:47]\n\tds_read_b64_tr_b16 v[118:119], %2 offset:544\n\t" \
  "v_mfma_f32_16x16x32_bf16 v[48:51], v[136:139], v[176:179], v[48:51]\n\tds_read_b64_tr_b16 v[120:121], %2 offset:256\n\t" \
  "buffer_load_dwordx4 v[164:167], %1, %[rx], %0 offen offset:2048 sc1\n\tv_mfma_f32_16x16x32_bf16 v[52:55], v[136:139], v[180:183], v[52:55]\n\t" \
  "ds_read_b64_tr_b16 v[122:123], %2 offset:768\n\tv_mfma_f32_16x16x32_bf16 v[56:59], v[140:143], v[176:179], v[56:59]\n\tds_read_b64_tr_b16 v[124:125], %2 offset:288\n\t" \
  "v_mfma_f32_16x16x32_bf16 v[60:63], v[140:143], v[180:183], v[60:63]\n\tds_read_b64_tr_b16 v[126:127], %2 offset:800\n\ts_waitcnt vmcnt(14) lgkmcnt(8)\n\t" \
  "v_mfma_f32_16x16x32_bf16 v[32:35], v[96:99], v[184:187], v[32:35]\n\tds_read_b64_tr_b16 v[128:129], %2 offset:4224\n\t" \
  "v_mfma_f32_16x16x32_bf16 v[36:39], v[96:99], v[188:191], v[36:39]\n\tds_read_b64_tr_b16 v[130:131], %2 offset:4736\n\t" \
  "buffer_load_dwordx4 v[168:171], %1, %[rx], %0 offen offset:1024 sc1\n\tv_mfma_f32_16x16x32_bf16 v[40:43], v[100:103], v[184:187], v[40:43]\n\t" \
  "ds_read_b64_tr_b16 v[132:133], %2 offset:4256\n\tv_mfma_f32_16x16x32_bf16 v[44:47], v[100:103], v[188:191], v[44:47]\n\tds_read_b64_tr_b16 v[134:135], %2 offset:4768\n\t" \
  "v_mfma_f32_16x16x32_bf16 v[48:51], v[104:107], v[184:187], v[48:51]\n\tds_read_b64_tr_b16 v[136:137], %2 offset:4480\n\t" \
  "buffer_load_dwordx4 v[172:175], %1, %[rx], %0 offen offset:3072 sc1\n\tv_mfma_f32_16x16x32_bf16 v[52:55], v[104:107], v[188:191], v[52:55]\n\t" \
  "ds_read_b64_tr_b16 v[138:139], %2 offset:4992\n\tv_mfma_f32_16x16x32_bf16 v[56:59], v[108:111], v[184:187], v[56:59]\n\tds_read_b64_tr_b16 v[140:141], %2 offset:4512\n\t" \
  "v_mfma_f32_16x16x32_bf16 v[60:63], v[108:111], v[188:191], v[60:63]\n\tds_read_b64_tr_b16 v[142:143], %2 offset:5024\n\ts_movk_i32 %[rem], 0x1000\n\t.Lop3%=:\n\t" \
  "s_and_b32 %0, %[m], %[rem]\n\ts_cbranch_scc0 .Los3%=\n\ts_movk_i32 %[c0], 12\n\ts_lshl_b32 %0, %[c0], 14\n\ts_xor_b32 %0, %0, %[x]\n\ts_waitcnt vmcnt(14) lgkmcnt(8)\n\t" \
  "v_mfma_f32_16x16x32_bf16 v[64:67], v[112:115], v[192:195], 0\n\tds_read_b64_tr_b16 v[96:97], %2 offset:8448\n\t" \
  "v_mfma_f32_16x16x32_bf16 v[68:71], v[112:115], v[196:199], 0\n\tds_read_b64_tr_b16 v[98:99], %2 offset:8960\n\t" \
  "buffer_load_dwordx4 v[176:179], %1, %[rx], %0 offen offset:0 sc1\n\tv_mfma_f32_16x16x32_bf16 v[72:75], v[116:119], v[192:195], 0\n\t" \
  "ds_read_b64_tr_b16 v[100:101], %2 offset:8480\n\ts_sub_u32 %[t], %[c0], %[c2]\n\tv_mfma_f32_16x16x32_bf16 v[76:79], v[116:119], v[196:199], 0\n\t" \
  "ds_read_b64_tr_b16 v[102:103], %2 offset:8992\n\ts_mul_i32 %[t], %[t], 8448\n\tv_mfma_f32_16x16x32_bf16 v[80:83], v[120:123], v[192:195], 0\n\t" \
  "ds_read_b64_tr_b16 v[104:105], %2 offset:8704\n\tbuffer_load_dwordx4 v[180:183], %1, %[rx], %0 offen offset:2048 sc1\n\t" \
  "v_mfma_f32_16x16x32_bf16 v[84:87], v[120:123], v[196:199], 0\n\tds_read_b64_tr_b16 v[106:107], %2 offset:9216\n\tv_add_u32 %3, %[t], %2\n\t" \
  "v_mfma_f32_16x16x32_bf16 v[88:91], v[124:127], v[192:195], 0\n\tds_read_b64_tr_b16 v[108:109], %2 offset:8736\n\t" \
  "v_mfma_f32_16x16x32_bf16 v[92:95], v[124:127], v[196:199], 0\n\tds_read_b64_tr_b16 v[110:111], %2 offset:9248\n\ts_waitcnt vmcnt(14) lgkmcnt(8)\n\t" \
  "v_mfma_f32_16x16x32_bf16 v[64:67], v[128:131], v[200:203], v[64:67]\n\tds_read_b64_tr_b16 v[112:113], %2 offset:12672\n\t" \
  "v_mfma_f32_16x16x32_bf16 v[68:71], v[128:131], v[204:207], v[68:71]\n\tds_read_b64_tr_b16 v[114:115], %2 offset:13184\n\t" \
  "buffer_load_dwordx4 v[184:187], %1, %[rx], %0 offen offset:1024 sc1\n\tv_mfma_f32_16x16x32_bf16 v[72:75], v[132:135], v[200:203], v[72:75]\n\t" \
  "ds_read_b64_tr_b16 v[116:117], %2 offset:12704\n\tv_mfma_f32_16x16x32_bf16 v[76:79], v[132:135], v[204:207], v[76:79]\n\t" \
  "ds_read_b64_tr_b16 v[118:119], %2 offset:13216\n\tv_mfma_f32_16x16x32_bf16 v[80:83], v[136:139], v[200:203], v[80:83]\n\t" \
  "ds_read_b64_tr_b16 v[120:121], %2 offset:12928\n\tbuffer_load_dwordx4 v[188:191], %1, %[rx], %0 offen offset:3072 sc1\n\t" \
  "v_mfma_f32_16x16x32_bf16 v[84:87], v[136:139], v[204:207], v[84:87]\n\tds_read_b64_tr_b16 v[122:123], %2 offset:13440\n\ts_xor_b32 %0, %0, 0x4000\n\t" \
  "v_mfma_f32_16x16x32_bf16 v[88:91], v[140:143], v[200:203], v[88:91]\n\tds_read_b64_tr_b16 v[124:125], %2 offset:12960\n\t" \
  "v_mfma_f32_16x16x32_bf16 v[92:95], v[140:143], v[204:207], v[92:95]\n\tds_read_b64_tr_b16 v[126:127], %2 offset:13472\n\ts_waitcnt vmcnt(14) lgkmcnt(8)\n\t" \
  "v_mfma_f32_16x16x32_bf16 v[64:67], v[96:99], v[208:211], v[64:67]\n\tds_read_b64_tr_b16 v[128:129], %2 offset:16896\n\t" \
  "v_mfma_f32_16x16x32_bf16 v[68:71], v[96:99], v[212:215], v[68:71]\n\tds_read_b64_tr_b16 v[130:131], %2 offset:17408\n\t" \
  "buffer_load_dwordx4 v[192:195], %1, %[rx], %0 offen offset:0 sc1\n\tv_mfma_f32_16x16x32_bf16 v[72:75], v[100:103], v[208:211], v[72:75]\n\t" \
  "ds_read_b64_tr_b16 v[132:133], %2 offset:16928\n\tv_mfma_f32_16x16x32_bf16 v[76:79], v[100:103], v[212:215], v[76:79]\n\t" \
  "ds_read_b64_tr_b16 v[134:135], %2 offset:17440\n\tv_mfma_f32_16x16x32_bf16 v[80:83], v[104:107], v[208:211], v[80:83]\n\t" \
  "ds_read_b64_tr_b16 v[136:137], %2 offset:17152\n\tbuffer_load_dwordx4 v[196:199], %1, %[rx], %0 offen offset:2048 sc1\n\t" \
  "v_mfma_f32_16x16x32_bf16 v[84:87], v[104:107], v[212:215], v[84:87]\n\tds_read_b64_tr_b16 v[138:139], %2 offset:17664\n\t" \
  "v_mfma_f32_16x16x32_bf16 v[88:91], v[108:111], v[208:211], v[88:91]\n\tds_read_b64_tr_b16 v[140:141], %2 offset:17184\n\t" \
  "v_mfma_f32_16x16x32_bf16 v[92:95], v[108:111], v[212:215], v[92:95]\n\tds_read_b64_tr_b16 v[142:143], %2 offset:17696\n\ts_waitcnt vmcnt(14) lgkmcnt(8)\n\t" \
  "v_mfma_f32_16x16x32_bf16 v[64:67], v[112:115], v[216:219], v[64:67]\n\tds_read_b64_tr_b16 v[96:97], %2 offset:21120\n\t" \
  "v_mfma_f32_16x16x32_bf16 v[68:71], v[112:115], v[220:223], v[68:71]\n\tds_read_b64_tr_b16 v[98:99], %2 offset:21632\n\t" \
  "buffer_load_dwordx4 v[200:203], %1, %[rx], %0 offen offset:1024 sc1\n\tv_mfma_f32_16x16x32_bf16 v[72:75], v[116:119], v[216:219], v[72:75]\n\t" \
  "ds_read_b64_tr_b16 v[100:101], %2 offset:21152\n\tv_mfma_f32_16x16x32_bf16 v[76:79], v[116:119], v[220:223], v[76:79]\n\t" \
  "ds_read_b64_tr_b16 v[102:103], %2 offset:21664\n\tv_mfma_f32_16x16x32_bf16 v[80:83], v[120:123], v[216:219], v[80:83]\n\t" \
  "ds_read_b64_tr_b16 v[104:105], %2 offset:21376\n\tbuffer_load_dwordx4 v[204:207], %1, %[rx], %0 offen offset:3072 sc1\n\t" \
  "v_mfma_f32_16x16x32_bf16 v[84:87], v[120:123], v[220:223], v[84:87]\n\tds_read_b64_tr_b16 v[106:107], %2 offset:21888\n\ts_xor_b32 %0, %0, 0xc000\n\t" \
  "v_mfma_f32_16x16x32_bf16 v[88:91], v[124:127], v[216:219], v[88:91]\n\tds_read_b64_tr_b16 v[108:109], %2 offset:21408\n\t" \
  "v_mfma_f32_16x16x32_bf16 v[92:95], v[124:127], v[220:223], v[92:95]\n\tds_read_b64_tr_b16 v[110:111], %2 offset:21920\n\ts_waitcnt vmcnt(14) lgkmcnt(8)\n\t" \
  "v_mfma_f32_16x16x32_bf16 v[64:67], v[128:131], v[144:147], v[64:67]\n\tds_read_b64_tr_b16 v[112:113], %2 offset:25344\n\t" \
  "v_mfma_f32_16x16x32_bf16 v[68:71], v[128:131], v[148:151], v[68:71]\n\tds_read_b64_tr_b16 v[114:115], %2 offset:25856\n\t" \
  "buffer_load_dwordx4 v[208:211], %1, %[rx], %0 offen offset:0 sc1\n\tv_mfma_f32_16x16x32_bf16 v[72:75], v[132:135], v[144:147], v[72:75]\n\t" \
  "ds_read_b64_tr_b16 v[116:117], %2 offset:25376\n\tv_mfma_f32_16x16x32_bf16 v[76:79], v[132:135], v[148:151], v[76:79]\n\t" \
  "ds_read_b64_tr_b16 v[118:119], %2 offset:25888\n\tv_mfma_f32_16x16x32_bf16 v[80:83], v[136:139], v[144:147], v[80:83]\n\t" \
  "ds_read_b64_tr_b16 v[120:121], %2 offset:25600\n\tbuffer_load_dwordx4 v[212:215], %1, %[rx], %0 offen offset:2048 sc1\n\t" \
  "v_mfma_f32_16x16x32_bf16 v[84:87], v[136:139], v[148:151], v[84:87]\n\tds_read_b64_tr_b16 v[122:123], %2 offset:26112\n\t" \
  "v_mfma_f32_16x16x32_bf16 v[88:91], v[140:143], v[144:147], v[88:91]\n\tds_read_b64_tr_b16 v[124:125], %2 offset:25632\n\t" \
  "v_mfma_f32_16x16x32_bf16 v[92:95], v[140:143], v[148:151], v[92:95]\n\tds_read_b64_tr_b16 v[126:127], %2 offset:26144\n\ts_waitcnt vmcnt(14) lgkmcnt(8)\n\t" \
  "v_mfma_f32_16x16x32_bf16 v[64:67], v[96:99], v[152:155], v[64:67]\n\tds_read_b64_tr_b16 v[128:129], %2 offset:29568\n\t" \
  "v_mfma_f32_16x16x32_bf16 v[68:71], v[96:99], v[156:159], v[68:71]\n\tds_read_b64_tr_b16 v[130:131], %2 offset:30080\n\t" \
  "buffer_load_dwordx4 v[216:219], %1, %[rx], %0 offen offset:1024 sc1\n\tv_mfma_f32_16x16x32_bf16 v[72:75], v[100:103], v[152:155], v[72:75]\n\t" \
  "ds_read_b64_tr_b16 v[132:133], %2 offset:29600\n\tv_mfma_f32_16x16x32_bf16 v[76:79], v[100:103], v[156:159], v[76:79]\n\t" \
  "ds_read_b64_tr_b16 v[134:135], %2 offset:30112\n\tv_mfma_f32_16x16x32_bf16 v[80:83], v[104:107], v[152:155], v[80:83]\n\t" \
  "ds_read_b64_tr_b16 v[136:137], %2 offset:29824\n\tbuffer_load_dwordx4 v[220:223], %1, %[rx], %0 offen offset:3072 sc1\n\t" \
  "v_mfma_f32_16x16x32_bf16 v[84:87], v[104:107], v[156:159], v[84:87]\n\tds_read_b64_tr_b16 v[138:139], %2 offset:30336\n\ts_xor_b32 %0, %0, 0x4000\n\t" \
  "v_mfma_f32_16x16x32_bf16 v[88:91], v[108:111], v[152:155], v[88:91]\n\tds_read_b64_tr_b16 v[140:141], %2 offset:29856\n\t" \
  "v_mfma_f32_16x16x32_bf16 v[92:95], v[108:111], v[156:159], v[92:95]\n\tds_read_b64_tr_b16 v[142:143], %2 offset:30368\n\ts_waitcnt vmcnt(14) lgkmcnt(8)\n\ts_barrier\n\t" \
  "v_mfma_f32_16x16x32_bf16 v[64:67], v[112:115], v[160:163], v[64:67]\n\tds_read_b64_tr_b16 v[96:97], %3 offset:0\n\t" \
  "v_mfma_f32_16x16x32_bf16 v[68:71], v[112:115], v[164:167], v[68:71]\n\tds_read_b64_tr_b16 v[98:99], %3 offset:512\n\t" \
  "buffer_load_dwordx4 v[144:147], %1, %[rx], %0 offen offset:0 sc1\n\tv_mfma_f32_16x16x32_bf16 v[72:75], v[116:119], v[160:163], v[72:75]\n\t" \
  "ds_read_b64_tr_b16 v[100:101], %3 offset:32\n\tv_mfma_f32_16x16x32_bf16 v[76:79], v[116:119], v[164:167], v[76:79]\n\tds_read_b64_tr_b16 v[102:103], %3 offset:544\n\t" \
  "v_mfma_f32_16x16x32_bf16 v[80:83], v[120:123], v[160:163], v[80:83]\n\tds_read_b64_tr_b16 v[104:105], %3 offset:256\n\t" \
  "buffer_load_dwordx4 v[148:151], %1, %[rx], %0 offen offset:2048 sc1\n\tv_mfma_f32_16x16x32_bf16 v[84:87], v[120:123], v[164:167], v[84:87]\n\t" \
  "ds_read_b64_tr_b16 v[106:107], %3 offset:768\n\tv_mfma_f32_16x16x32_bf16 v[88:91], v[124:127], v[160:163], v[88:91]\n\tds_read_b64_tr_b16 v[108:109], %3 offset:288\n\t" \
  "v_mfma_f32_16x16x32_bf16 v[92:95], v[124:127], v[164:167], v[92:95]\n\tds_read_b64_tr_b16 v[110:111], %3 offset:800\n\ts_waitcnt vmcnt(14) lgkmcnt(8)\n\t" \
  "v_mfma_f32_16x16x32_bf16 v[64:67], v[128:131], v[168:171], v[64:67]\n\tds_read_b64_tr_b16 v[112:113], %3 offset:4224\n\t" \
  "v_mfma_f32_16x16x32_bf16 v[68:71], v[128:131], v[172:175], v[68:71]\n\tds_read_b64_tr_b16 v[114:115], %3 offset:4736\n\t" \
  "buffer_load_dwordx4 v[152:155], %1, %[rx], %0 offen offset:1024 sc1\n\tv_mfma_f32_16x16x32_bf16 v[72:75], v[132:135], v[168:171], v[72:75]\n\t" \
  "ds_read_b64_tr_b16 v[116:117], %3 offset:4256\n\tv_mfma_f32_16x16x32_bf16 v[76:79], v[132:135], v[172:175], v[76:79]\n\tds_read_b64_tr_b16 v[118:119], %3 offset:4768\n\t" \
  "v_mfma_f32_16x16x32_bf16 v[80:83], v[136:139], v[168:171], v[80:83]\n\tds_read_b64_tr_b16 v[120:121], %3 offset:4480\n\t" \
  "buffer_load_dwordx4 v[156:159], %1, %[rx], %0 offen offset:3072 sc1\n\tv_mfma_f32_16x16x32_bf16 v[84:87], v[136:139], v[172:175], v[84:87]\n\t" \
  "ds_read_b64_tr_b16 v[122:123], %3 offset:4992\n\tv_mfma_f32_16x16x32_bf16 v[88:91], v[140:143], v[168:171], v[88:91]\n\tds_read_b64_tr_b16 v[124:125], %3 offset:4512\n\t" \
  "v_mfma_f32_16x16x32_bf16 v[92:95], v[140:143], v[172:175], v[92:95]\n\tds_read_b64_tr_b16 v[126:127], %3 offset:5024\n\t" \
  "buffer_load_dwordx4 v[160:163], %4, %[rr], %[r0] offen sc1\n\tbuffer_load_dwordx4 v[164:167], %4, %[rr], %[r1] offen sc1\n\t" \
  "buffer_load_dwordx4 v[168:171], %4, %[rr], %[r2] offen sc1\n\tbuffer_load_dwordx4 v[172:175], %4, %[rr], %[r3] offen sc1\n\ts_waitcnt vmcnt(18) lgkmcnt(8)\n\t" \
  "s_barrier\n\ts_nop 7\n\ts_cmp_eq_u32 %[c2], 8\n\ts_cbranch_scc0 .Loa%=\n\tv_pk_add_f32 v[0:1], v[0:1], v[32:33]\n\tv_pk_add_f32 v[2:3], v[2:3], v[34:35]\n\t" \
  "v_pk_add_f32 v[4:5], v[4:5], v[36:37]\n\tv_pk_add_f32 v[6:7], v[6:7], v[38:39]\n\tv_pk_add_f32 v[8:9], v[8:9], v[40:41]\n\tv_pk_add_f32 v[10:11], v[10:11], v[42:43]\n\t" \
  "v_pk_add_f32 v[12:13], v[12:13], v[44:45]\n\tv_pk_add_f32 v[14:15], v[14:15], v[46:47]\n\tv_pk_add_f32 v[16:17], v[16:17], v[48:49]\n\t" \
  "v_pk_add_f32 v[18:19], v[18:19], v[50:51]\n\tv_pk_add_f32 v[20:21], v[20:21], v[52:53]\n\tv_pk_add_f32 v[22:23], v[22:23], v[54:55]\n\t" \
  "v_pk_add_f32 v[24:25], v[24:25], v[56:57]\n\tv_pk_add_f32 v[26:27], v[26:27], v[58:59]\n\tv_pk_add_f32 v[28:29], v[28:29], v[60:61]\n\t" \
  "v_pk_add_f32 v[30:31], v[30:31], v[62:63]\n\tv_pk_add_f32 v[0:1], v[0:1], v[64:65]\n\tv_pk_add_f32 v[2:3], v[2:3], v[66:67]\n\tv_pk_add_f32 v[4:5], v[4:5], v[68:69]\n\t" \
  "v_pk_add_f32 v[6:7], v[6:7], v[70:71]\n\tv_pk_add_f32 v[8:9], v[8:9], v[72:73]\n\tv_pk_add_f32 v[10:11], v[10:11], v[74:75]\n\t" \
  "v_pk_add_f32 v[12:13], v[12:13], v[76:77]\n\tv_pk_add_f32 v[14:15], v[14:15], v[78:79]\n\tv_pk_add_f32 v[16:17], v[16:17], v[80:81]\n\t" \
  "v_pk_add_f32 v[18:19], v[18:19], v[82:83]\n\tv_pk_add_f32 v[20:21], v[20:21], v[84:85]\n\tv_pk_add_f32 v[22:23], v[22:23], v[86:87]\n\t" \
  "v_pk_add_f32 v[24:25], v[24:25], v[88:89]\n\tv_pk_add_f32 v[26:27], v[26:27], v[90:91]\n\tv_pk_add_f32 v[28:29], v[28:29], v[92:93]\n\t" \
  "v_pk_add_f32 v[30:31], v[30:31], v[94:95]\n\t.Loc%=:\n\tv_mfma_f32_16x16x32_bf16 v[0:3], v[96:99], v[176:179], v[0:3]\n\tds_read_b64_tr_b16 v[128:129], %3 offset:8448\n\t" \
  "v_mfma_f32_16x16x32_bf16 v[4:7], v[96:99], v[180:183], v[4:7]\n\tds_read_b64_tr_b16 v[130:131], %3 offset:8960\n\t" \
  "v_mfma_f32_16x16x32_bf16 v[8:11], v[100:103], v[176:179], v[8:11]\n\tds_read_b64_tr_b16 v[132:133], %3 offset:8480\n\t" \
  "v_mfma_f32_16x16x32_bf16 v[12:15], v[100:103], v[180:183], v[12:15]\n\tds_read_b64_tr_b16 v[134:135], %3 offset:8992\n\t" \
  "v_mfma_f32_16x16x32_bf16 v[16:19], v[104:107], v[176:179], v[16:19]\n\tds_read_b64_tr_b16 v[136:137], %3 offset:8704\n\t" \
  "v_mfma_f32_16x16x32_bf16 v[20:23], v[104:107], v[180:183], v[20:23]\n\tds_read_b64_tr_b16 v[138:139], %3 offset:9216\n\t" \
  "v_mfma_f32_16x16x32_bf16 v[24:27], v[108:111], v[176:179], v[24:27]\n\tds_read_b64_tr_b16 v[140:141], %3 offset:8736\n\t" \
  "v_mfma_f32_16x16x32_bf16 v[28:31], v[108:111], v[180:183], v[28:31]\n\tds_read_b64_tr_b16 v[142:143], %3 offset:9248\n\ts_waitcnt vmcnt(16) lgkmcnt(8)\n\t" \
  "v_mfma_f32_16x16x32_bf16 v[0:3], v[112:115], v[184:187], v[0:3]\n\tds_read_b64_tr_b16 v[96:97], %3 offset:12672\n\t" \
  "v_mfma_f32_16x16x32_bf16 v[4:7], v[112:115], v[188:191], v[4:7]\n\tds_read_b64_tr_b16 v[98:99], %3 offset:13184\n\t" \
  "v_mfma_f32_16x16x32_bf16 v[8:11], v[116:119], v[184:187], v[8:11]\n\tds_read_b64_tr_b16 v[100:101], %3 offset:12704\n\t" \
  "v_mfma_f32_16x16x32_bf16 v[12:15], v[116:119], v[188:191], v[12:15]\n\tds_read_b64_tr_b16 v[102:103], %3 offset:13216\n\t" \
  "v_mfma_f32_16x16x32_bf16 v[16:19], v[120:123], v[184:187], v[16:19]\n\tds_read_b64_tr_b16 v[104:105], %3 offset:12928\n\t" \
  "v_mfma_f32_16x16x32_bf16 v[20:23], v[120:123], v[188:191], v[20:23]\n\tds_read_b64_tr_b16 v[106:107], %3 offset:13440\n\t" \
  "v_mfma_f32_16x16x32_bf16 v[24:27], v[124:127], v[184:187], v[24:27]\n\tds_read_b64_tr_b16 v[108:109], %3 offset:12960\n\t" \
  "v_mfma_f32_16x16x32_bf16 v[28:31], v[124:127], v[188:191], v[28:31]\n\tds_read_b64_tr_b16 v[110:111], %3 offset:13472\n\ts_waitcnt vmcnt(14) lgkmcnt(8)\n\t" \
  "v_mfma_f32_16x16x32_bf16 v[0:3], v[128:131], v[192:195], v[0:3]\n\tds_read_b64_tr_b16 v[112:113], %3 offset:16896\n\t" \
  "v_mfma_f32_16x16x32_bf16 v[4:7], v[128:131], v[196:199], v[4:7]\n\tds_read_b64_tr_b16 v[114:115], %3 offset:17408\n\t" \
  "v_mfma_f32_16x16x32_bf16 v[8:11], v[132:135], v[192:195], v[8:11]\n\tds_read_b64_tr_b16 v[116:117], %3 offset:16928\n\t" \
  "v_mfma_f32_16x16x32_bf16 v[12:15], v[132:135], v[196:199], v[12:15]\n\tds_read_b64_tr_b16 v[118:119], %3 offset:17440\n\t" \
  "v_mfma_f32_16x16x32_bf16 v[16:19], v[136:139], v[192:195], v[16:19]\n\tds_read_b64_tr_b16 v[120:121], %3 offset:17152\n\t" \
  "v_mfma_f32_16x16x32_bf16 v[20:23], v[136:139], v[196:199], v[20:23]\n\tds_read_b64_tr_b16 v[122:123], %3 offset:17664\n\t" \
  "v_mfma_f32_16x16x32_bf16 v[24:27], v[140:143], v[192:195], v[24:27]\n\tds_read_b64_tr_b16 v[124:125], %3 offset:17184\n\t" \
  "v_mfma_f32_16x16x32_bf16 v[28:31], v[140:143], v[196:199], v[28:31]\n\tds_read_b64_tr_b16 v[126:127], %3 offset:17696\n\ts_waitcnt vmcnt(12) lgkmcnt(8)\n\t" \
  "v_mfma_f32_16x16x32_bf16 v[0:3], v[96:99], v[200:203], v[0:3]\n\tds_read_b64_tr_b16 v[128:129], %3 offset:21120\n\t" \
  "v_mfma_f32_16x16x32_bf16 v[4:7], v[96:99], v[204:207], v[4:7]\n\tds_read_b64_tr_b16 v[130:131], %3 offset:21632\n\t" \
  "v_mfma_f32_16x16x32_bf16 v[8:11], v[100:103], v[200:203], v[8:11]\n\tds_read_b64_tr_b16 v[132:133], %3 offset:21152\n\t" \
  "v_mfma_f32_16x16x32_bf16 v[12:15], v[100:103], v[204:207], v[12:15]\n\tds_read_b64_tr_b16 v[134:135], %3 offset:21664\n\t" \
  "v_mfma_f32_16x16x32_bf16 v[16:19], v[104:107], v[200:203], v[16:19]\n\tds_read_b64_tr_b16 v[136:137], %3 offset:21376\n\t" \
  "v_mfma_f32_16x16x32_bf16 v[20:23], v[104:107], v[204:207], v[20:23]\n\tds_read_b64_tr_b16 v[138:139], %3 offset:21888\n\t" \
  "v_mfma_f32_16x16x32_bf16 v[24:27], v[108:111], v[200:203], v[24:27]\n\tds_read_b64_tr_b16 v[140:141], %3 offset:21408\n\t" \
  "v_mfma_f32_16x16x32_bf16 v[28:31], v[108:111], v[204:207], v[28:31]\n\tds_read_b64_tr_b16 v[142:143], %3 offset:21920\n\ts_waitcnt vmcnt(10) lgkmcnt(8)\n\t" \
  "v_mfma_f32_16x16x32_bf16 v[0:3], v[112:115], v[208:211], v[0:3]\n\tds_read_b64_tr_b16 v[96:97], %3 offset:25344\n\t" \
  "v_mfma_f32_16x16x32_bf16 v[4:7], v[112:115], v[212:215], v[4:7]\n\tds_read_b64_tr_b16 v[98:99], %3 offset:25856\n\t" \
  "v_mfma_f32_16x16x32_bf16 v[8:11], v[116:119], v[208:211], v[8:11]\n\tds_read_b64_tr_b16 v[100:101], %3 offset:25376\n\t" \
  "v_mfma_f32_16x16x32_bf16 v[12:15], v[116:119], v[212:215], v[12:15]\n\tds_read_b64_tr_b16 v[102:103], %3 offset:25888\n\t" \
  "v_mfma_f32_16x16x32_bf16 v[16:19], v[120:123], v[208:211], v[16:19]\n\tds_read_b64_tr_b16 v[104:105], %3 offset:25600\n\t" \
  "v_mfma_f32_16x16x32_bf16 v[20:23], v[120:123], v[212:215], v[20:23]\n\tds_read_b64_tr_b16 v[106:107], %3 offset:26112\n\t" \
  "v_mfma_f32_16x16x32_bf16 v[24:27], v[124:127], v[208:211], v[24:27]\n\tds_read_b64_tr_b16 v[108:109], %3 offset:25632\n\t" \
  "v_mfma_f32_16x16x32_bf16 v[28:31], v[124:127], v[212:215], v[28:31]\n\tds_read_b64_tr_b16 v[110:111], %3 offset:26144\n\ts_waitcnt vmcnt(8) lgkmcnt(8)\n\t" \
  "v_mfma_f32_16x16x32_bf16 v[0:3], v[128:131], v[216:219], v[0:3]\n\tds_read_b64_tr_b16 v[112:113], %3 offset:29568\n\t" \
  "v_mfma_f32_16x16x32_bf16 v[4:7], v[128:131], v[220:223], v[4:7]\n\tds_read_b64_tr_b16 v[114:115], %3 offset:30080\n\t" \
  "v_mfma_f32_16x16x32_bf16 v[8:11], v[132:135], v[216:219], v[8:11]\n\tds_read_b64_tr_b16 v[116:117], %3 offset:29600\n\t" \
  "v_mfma_f32_16x16x32_bf16 v[12:15], v[132:135], v[220:223], v[12:15]\n\tds_read_b64_tr_b16 v[118:119], %3 offset:30112\n\t" \
  "v_mfma_f32_16x16x32_bf16 v[16:19], v[136:139], v[216:219], v[16:19]\n\tds_read_b64_tr_b16 v[120:121], %3 offset:29824\n\t" \
  "v_mfma_f32_16x16x32_bf16 v[20:23], v[136:139], v[220:223], v[20:23]\n\tds_read_b64_tr_b16 v[122:123], %3 offset:30336\n\t" \
  "v_mfma_f32_16x16x32_bf16 v[24:27], v[140:143], v[216:219], v[24:27]\n\tds_read_b64_tr_b16 v[124:125], %3 offset:29856\n\t" \
  "v_mfma_f32_16x16x32_bf16 v[28:31], v[140:143], v[220:223], v[28:31]\n\tds_read_b64_tr_b16 v[126:127], %3 offset:30368\n\ts_waitcnt vmcnt(6) lgkmcnt(8)\n\t" \
  "v_mfma_f32_16x16x32_bf16 v[0:3], v[96:99], v[144:147], v[0:3]\n\tv_mfma_f32_16x16x32_bf16 v[4:7], v[96:99], v[148:151], v[4:7]\n\t" \
  "v_mfma_f32_16x16x32_bf16 v[8:11], v[100:103], v[144:147], v[8:11]\n\tv_mfma_f32_16x16x32_bf16 v[12:15], v[100:103], v[148:151], v[12:15]\n\t" \
  "v_mfma_f32_16x16x32_bf16 v[16:19], v[104:107], v[144:147], v[16:19]\n\tv_mfma_f32_16x16x32_bf16 v[20:23], v[104:107], v[148:151], v[20:23]\n\t" \
  "v_mfma_f32_16x16x32_bf16 v[24:27], v[108:111], v[144:147], v[24:27]\n\tv_mfma_f32_16x16x32_bf16 v[28:31], v[108:111], v[148:151], v[28:31]\n\t" \
  "s_waitcnt vmcnt(4) lgkmcnt(0)\n\tv_mfma_f32_16x16x32_bf16 v[0:3], v[112:115], v[152:155], v[0:3]\n\tv_mfma_f32_16x16x32_bf16 v[4:7], v[112:115], v[156:159], v[4:7]\n\t" \
  "v_mfma_f32_16x16x32_bf16 v[8:11], v[116:119], v[152:155], v[8:11]\n\tv_mfma_f32_16x16x32_bf16 v[12:15], v[116:119], v[156:159], v[12:15]\n\t" \
  "v_mfma_f32_16x16x32_bf16 v[16:19], v[120:123], v[152:155], v[16:19]\n\tv_mfma_f32_16x16x32_bf16 v[20:23], v[120:123], v[156:159], v[20:23]\n\t" \
  "v_mfma_f32_16x16x32_bf16 v[24:27], v[124:127], v[152:155], v[24:27]\n\tv_mfma_f32_16x16x32_bf16 v[28:31], v[124:127], v[156:159], v[28:31]\n\ts_waitcnt vmcnt(0)\n\t" \
  "v_mfma_f32_16x16x32_bf16 v[0:3], v[224:227], v[160:163], v[0:3]\n\tv_mfma_f32_16x16x32_bf16 v[4:7], v[224:227], v[168:171], v[4:7]\n\t" \
  "v_mfma_f32_16x16x32_bf16 v[8:11], v[228:231], v[160:163], v[8:11]\n\tv_mfma_f32_16x16x32_bf16 v[12:15], v[228:231], v[168:171], v[12:15]\n\t" \
  "v_mfma_f32_16x16x32_bf16 v[16:19], v[224:227], v[164:167], v[16:19]\n\tv_mfma_f32_16x16x32_bf16 v[20:23], v[224:227], v[172:175], v[20:23]\n\t" \
  "v_mfma_f32_16x16x32_bf16 v[24:27], v[228:231], v[164:167], v[24:27]\n\tv_mfma_f32_16x16x32_bf16 v[28:31], v[228:231], v[172:175], v[28:31]\n\ts_branch .Loe%=\n\t" \
  ".Lon%=:\n\ts_waitcnt vmcnt(0)\n\ts_barrier\n\t.Lop0%=:\n\ts_and_b32 %0, %[m], %[rem]\n\ts_cbranch_scc0 .Los0%=\n\ts_and_b32 %[t], %0, %[hm]\n\t" \
  "s_cselect_b32 %0, %[t], %0\n\ts_ff1_i32_b32 %[c0], %0\n\ts_bitset0_b32 %[rem], %[c0]\n\ts_lshl_b32 %0, %[c0], 14\n\ts_xor_b32 %0, %0, %[x]\n\t" \
  "buffer_load_dwordx4 v[144:147], %1, %[rx], %0 offen offset:0 sc1\n\tbuffer_load_dwordx4 v[148:151], %1, %[rx], %0 offen offset:2048 sc1\n\t" \
  "buffer_load_dwordx4 v[152:155], %1, %[rx], %0 offen offset:1024 sc1\n\tbuffer_load_dwordx4 v[156:159], %1, %[rx], %0 offen offset:3072 sc1\n\ts_xor_b32 %0, %0, 0x4000\n\t" \
  "buffer_load_dwordx4 v[160:163], %1, %[rx], %0 offen offset:0 sc1\n\tbuffer_load_dwordx4 v[164:167], %1, %[rx], %0 offen offset:2048 sc1\n\t" \
  "buffer_load_dwordx4 v[168:171], %1, %[rx], %0 offen offset:1024 sc1\n\tbuffer_load_dwordx4 v[172:175], %1, %[rx], %0 offen offset:3072 sc1\n\ts_xor_b32 %0, %0, 0xc000\n\t" \
  "buffer_load_dwordx4 v[176:179], %1, %[rx], %0 offen offset:0 sc1\n\tbuffer_load_dwordx4 v[180:183], %1, %[rx], %0 offen offset:2048 sc1\n\t" \
  "buffer_load_dwordx4 v[184:187], %1, %[rx], %0 offen offset:1024 sc1\n\tbuffer_load_dwordx4 v[188:191], %1, %[rx], %0 offen offset:3072 sc1\n\ts_xor_b32 %0, %0, 0x4000\n\t" \
  "buffer_load_dwordx4 v[192:195], %1, %[rx], %0 offen offset:0 sc1\n\tbuffer_load_dwordx4 v[196:199], %1, %[rx], %0 offen offset:2048 sc1\n\t" \
  "buffer_load_dwordx4 v[200:203], %1, %[rx], %0 offen offset:1024 sc1\n\tbuffer_load_dwordx4 v[204:207], %1, %[rx], %0 offen offset:3072 sc1\n\t" \
  "s_mul_i32 %[t], %[c0], 8448\n\tv_add_u32 %2, %[t], %2\n\tds_read_b64_tr_b16 v[96:97], %2 offset:0\n\tds_read_b64_tr_b16 v[98:99], %2 offset:512\n\t" \
  "ds_read_b64_tr_b16 v[100:101], %2 offset:32\n\tds_read_b64_tr_b16 v[102:103], %2 offset:544\n\tds_read_b64_tr_b16 v[104:105], %2 offset:256\n\t" \
  "ds_read_b64_tr_b16 v[106:107], %2 offset:768\n\tds_read_b64_tr_b16 v[108:109], %2 offset:288\n\tds_read_b64_tr_b16 v[110:111], %2 offset:800\n\t" \
  "ds_read_b64_tr_b16 v[112:113], %2 offset:4224\n\tds_read_b64_tr_b16 v[114:115], %2 offset:4736\n\tds_read_b64_tr_b16 v[116:117], %2 offset:4256\n\t" \
  "ds_read_b64_tr_b16 v[118:119], %2 offset:4768\n\tds_read_b64_tr_b16 v[120:121], %2 offset:4480\n\tds_read_b64_tr_b16 v[122:123], %2 offset:4992\n\t" \
  "ds_read_b64_tr_b16 v[124:125], %2 offset:4512\n\tds_read_b64_tr_b16 v[126:127], %2 offset:5024\n\ts_branch .Log%=\n\t.Los0%=:\n\tv_mov_b32 v208, %[om]\n\t" \
  "ds_read_b32 v208, v208\n\ts_waitcnt lgkmcnt(0)\n\tv_readfirstlane_b32 %0, v208\n\ts_nop 0\n\ts_or_b32 %[m], %[m], %0\n\ts_branch .Lop0%=\n\t.Los1%=:\n\t" \
  "v_mov_b32 v208, %[om]\n\tds_read_b32 v208, v208\n\ts_waitcnt lgkmcnt(0)\n\tv_readfirstlane_b32 %0, v208\n\ts_nop 0\n\ts_or_b32 %[m], %[m], %0\n\ts_branch .Lop1%=\n\t" \
  ".Los2%=:\n\tv_mov_b32 v192, %[om]\n\tds_read_b32 v192, v192\n\ts_waitcnt lgkmcnt(0)\n\tv_readfirstlane_b32 %0, v192\n\ts_nop 0\n\ts_or_b32 %[m], %[m], %0\n\t" \
  "s_branch .Lop2%=\n\t.Los3%=:\n\tv_mov_b32 v176, %[om]\n\tds_read_b32 v176, v176\n\ts_waitcnt lgkmcnt(0)\n\tv_readfirstlane_b32 %0, v176\n\ts_nop 0\n\t" \
  "s_or_b32 %[m], %[m], %0\n\ts_branch .Lop3%=\n\t.Loa%=:\n\ts_cmp_eq_u32 %[c1], 8\n\ts_cbranch_scc0 .Lob%=\n\tv_pk_add_f32 v[0:1], v[0:1], v[64:65]\n\t" \
  "v_pk_add_f32 v[2:3], v[2:3], v[66:67]\n\tv_pk_add_f32 v[4:5], v[4:5], v[68:69]\n\tv_pk_add_f32 v[6:7], v[6:7], v[70:71]\n\tv_pk_add_f32 v[8:9], v[8:9], v[72:73]\n\t" \
  "v_pk_add_f32 v[10:11], v[10:11], v[74:75]\n\tv_pk_add_f32 v[12:13], v[12:13], v[76:77]\n\tv_pk_add_f32 v[14:15], v[14:15], v[78:79]\n\t" \
  "v_pk_add_f32 v[16:17], v[16:17], v[80:81]\n\tv_pk_add_f32 v[18:19], v[18:19], v[82:83]\n\tv_pk_add_f32 v[20:21], v[20:21], v[84:85]\n\t" \
  "v_pk_add_f32 v[22:23], v[22:23], v[86:87]\n\tv_pk_add_f32 v[24:25], v[24:25], v[88:89]\n\tv_pk_add_f32 v[26:27], v[26:27], v[90:91]\n\t" \
  "v_pk_add_f32 v[28:29], v[28:29], v[92:93]\n\tv_pk_add_f32 v[30:31], v[30:31], v[94:95]\n\tv_pk_add_f32 v[0:1], v[0:1], v[32:33]\n\t" \
  "v_pk_add_f32 v[2:3], v[2:3], v[34:35]\n\tv_pk_add_f32 v[4:5], v[4:5], v[36:37]\n\tv_pk_add_f32 v[6:7], v[6:7], v[38:39]\n\tv_pk_add_f32 v[8:9], v[8:9], v[40:41]\n\t" \
  "v_pk_add_f32 v[10:11], v[10:11], v[42:43]\n\tv_pk_add_f32 v[12:13], v[12:13], v[44:45]\n\tv_pk_add_f32 v[14:15], v[14:15], v[46:47]\n\t" \
  "v_pk_add_f32 v[16:17], v[16:17], v[48:49]\n\tv_pk_add_f32 v[18:19], v[18:19], v[50:51]\n\tv_pk_add_f32 v[20:21], v[20:21], v[52:53]\n\t" \
  "v_pk_add_f32 v[22:23], v[22:23], v[54:55]\n\tv_pk_add_f32 v[24:25], v[24:25], v[56:57]\n\tv_pk_add_f32 v[26:27], v[26:27], v[58:59]\n\t" \
  "v_pk_add_f32 v[28:29], v[28:29], v[60:61]\n\tv_pk_add_f32 v[30:31], v[30:31], v[62:63]\n\ts_branch .Loc%=\n\t.Lob%=:\n\tv_pk_add_f32 v[32:33], v[32:33], v[64:65]\n\t" \
  "v_pk_add_f32 v[34:35], v[34:35], v[66:67]\n\tv_pk_add_f32 v[36:37], v[36:37], v[68:69]\n\tv_pk_add_f32 v[38:39], v[38:39], v[70:71]\n\t" \
  "v_pk_add_f32 v[40:41], v[40:41], v[72:73]\n\tv_pk_add_f32 v[42:43], v[42:43], v[74:75]\n\tv_pk_add_f32 v[44:45], v[44:45], v[76:77]\n\t" \
  "v_pk_add_f32 v[46:47], v[46:47], v[78:79]\n\tv_pk_add_f32 v[48:49], v[48:49], v[80:81]\n\tv_pk_add_f32 v[50:51], v[50:51], v[82:83]\n\t" \
  "v_pk_add_f32 v[52:53], v[52:53], v[84:85]\n\tv_pk_add_f32 v[54:55], v[54:55], v[86:87]\n\tv_pk_add_f32 v[56:57], v[56:57], v[88:89]\n\t" \
  "v_pk_add_f32 v[58:59], v[58:59], v[90:91]\n\tv_pk_add_f32 v[60:61], v[60:61], v[92:93]\n\tv_pk_add_f32 v[62:63], v[62:63], v[94:95]\n\t" \
  "v_pk_add_f32 v[0:1], v[0:1], v[32:33]\n\tv_pk_add_f32 v[2:3], v[2:3], v[34:35]\n\tv_pk_add_f32 v[4:5], v[4:5], v[36:37]\n\tv_pk_add_f32 v[6:7], v[6:7], v[38:39]\n\t" \
  "v_pk_add_f32 v[8:9], v[8:9], v[40:41]\n\tv_pk_add_f32 v[10:11], v[10:11], v[42:43]\n\tv_pk_add_f32 v[12:13], v[12:13], v[44:45]\n\t" \
  "v_pk_add_f32 v[14:15], v[14:15], v[46:47]\n\tv_pk_add_f32 v[16:17], v[16:17], v[48:49]\n\tv_pk_add_f32 v[18:19], v[18:19], v[50:51]\n\t" \
  "v_pk_add_f32 v[20:21], v[20:21], v[52:53]\n\tv_pk_add_f32 v[22:23], v[22:23], v[54:55]\n\tv_pk_add_f32 v[24:25], v[24:25], v[56:57]\n\t" \
  "v_pk_add_f32 v[26:27], v[26:27], v[58:59]\n\tv_pk_add_f32 v[28:29], v[28:29], v[60:61]\n\tv_pk_add_f32 v[30:31], v[30:31], v[62:63]\n\ts_branch .Loc%=\n\t.Loe%=:\n\t" \
  "s_nop 7\n\ts_nop 7\n\t"
#define OPROJ_CLOB \
  "v32", "v33", "v34", "v35", "v36", "v37", "v38", "v39", "v40", "v41", "v42", "v43", "v44", "v45", "v46", "v47", "v48", "v49", "v50", "v51", "v52", "v53", "v54", "v55", \
  "v56", "v57", "v58", "v59", "v60", "v61", "v62", "v63", "v64", "v65", "v66", "v67", "v68", "v69", "v70", "v71", "v72", "v73", "v74", "v75", "v76", "v77", "v78", "v79", \
  "v80", "v81", "v82", "v83", "v84", "v85", "v86", "v87", "v88", "v89", "v90", "v91", "v92", "v93", "v94", "v95", "v128", "v129", "v130", "v131", "v132", "v133", "v134", \
  "v135", "v136", "v137", "v138", "v139", "v140", "v141", "v142", "v143", "v208", "v209", "v210", "v211", "v212", "v213", "v214", "v215", "v216", "v217", "v218", "v219", \
  "v220", "v221", "v222", "v223"

constexpr int PIECE = 1056, WTILE = 8 * PIECE, L_XCH = 12 * WTILE, L_Q = 131072, L_KV = 65536, L_BI = L_XCH + 53760, L_CS = L_BI + 1024, L_B2 = L_CS + 2048, L_DUMMY = L_B2 + 256, L_TAG = L_DUMMY + 256, L_STAMP = L_TAG + 16, L_DBG = L_STAMP + 512, L_END = L_DBG + 256;
typedef unsigned long long u64;
typedef __attribute__((address_space(1))) u64 gu64;
constexpr int SPINS = 1 << 17;
#define PMASK 1
#define PSL0 0
#define PSLP 1
typedef unsigned u32x8 __attribute__((ext_vector_type(8)));
struct Pub {
  rsrc_t r; unsigned v; u32x2 tag;
  DI Pub(lds_ptr lds, char* fl, int own, int line, int word) {
    r = mkrsrc(fl + own * 512 + line * 128 + word * 8, 8); v = lane_id() * 8; tag = *(const LAS u32x2*)(lds + L_TAG);
    asm volatile("" : "+v"(v), "+v"(tag));
    asm volatile("" : "+s"(r));
  }
  template <int N> DI void go() const { asm volatile("s_waitcnt vmcnt(%3)\n\tbuffer_store_dwordx2 %0, %1, %2, 0 offen sc1" ::"v"(tag), "v"(v), "s"(r), "i"(N) : "memory"); }
};
template <int WORDS> struct Flags {
  rsrc_t r; unsigned v; u64 tag;
  DI Flags(lds_ptr lds, char* fl, int group, int line) {
    const int lane = lane_id();
    r = mkrsrc(fl + group * 8192 + line * 128, 8192);
    v = WORDS == 1 ? lane * 512 : (lane & 47) * 512 + (lane & 16) / 2;
    tag = *(const LAS u64*)(lds + L_TAG);
    asm volatile("" : "+v"(v), "+v"(tag));
    asm volatile("" : "+s"(r));
  }
  DI void await() const {
    unsigned n; u64 f;
    asm volatile("s_mov_b32 %1, 0x20000\n\ts_mov_b64 exec, %5\n\ts_sleep %7\n"
                 ".Lpl%=:\n\tbuffer_load_dwordx2 %0, %2, %3, 0 offen sc1\n\ts_waitcnt vmcnt(0)\n\t"
                 "v_cmpx_ne_u64 vcc, %0, %4\n\ts_cbranch_execz .Lpe%=\n\t"
                 "s_sleep %6\n\ts_sub_u32 %1, %1, 1\n\ts_cbranch_scc0 .Lpl%=\n.Lpe%=:\n\ts_mov_b64 exec, -1"
                 : "=&v"(f), "=&s"(n) : "v"(v), "s"(r), "v"(tag), "s"(WORDS == 1 ? 0xffffull : 0xffffffffull), "i"(PSLP), "i"(PSL0) : "memory", "vcc", "scc");
  }
};

typedef float f32x32 __attribute__((ext_vector_type(32)));
template <int MODE  > struct Big {
  static constexpr int LDW = MODE == 0 ? 3072 : F_;
  static constexpr int XB = 16384, WB = 24 * PIECE, EB = MODE == 1 ? 4 * PIECE : 0, SLOT = XB + WB + EB, NS = 3, NT = 16;
  static constexpr int TCS = L_CS + MODE * 1024, TRS = NS * SLOT, TMR = TRS + 512;
  int mt, nt, seg[3], ecol;
  DI Big() {
    const int b = block_id(), xcd = b & 7, idx = b >> 3;
    if (MODE == 0) {
      mt = (xcd & 1) * 8 + (idx & 7); nt = NTMAP;
      if (MAPQ == 3) { mt = (xcd & 3) * 4 + (idx & 3); nt = (xcd >> 2) * 8 + (idx >> 2); }
      if (MAPQ == 4) { mt = xcd * 2 + (idx & 1); nt = idx >> 1; }
      seg[0] = nt * 64; seg[1] = 1024 + nt * 64; seg[2] = 2048 + nt * 64; ecol = 0;
    } else {
      mt = FMT; nt = (xcd >> 2) * 8 + (idx >> 2);
      seg[0] = (nt >> 1) * 448 + (nt & 1) * 256; seg[1] = seg[0] + 64; seg[2] = seg[0] + 128; ecol = (nt >> 1) * 448 + 192 + (nt & 1) * 32;
    }
  }
  DI int col(int w) const { return MODE == 0 ? w * 1024 + nt * 64 : seg[0] + w * 64 - (w == 3 ? (nt & 1) * 224 : 0); }
};
#define QBAR 1
#define HF_N 8
#define PRE_N 10
static_assert(HF_N >= 8, "");
#define TLS 57344
#define TLN 64
#define CSLP 0
#define LPRIO 0
#define CSPL 0
#define WARMP 3
#define STAG 0
struct Ffn2 {
  int mt, np, kh, nt;
  DI Ffn2() { const int b = block_id(), xcd = b & 7, idx = b >> 3; mt = FMT; np = idx >> 2; kh = xcd >> 2; nt = np * 2 + kh; }
  DI int prod(int k) const { return (np + k * ((mt & STAG) * 2 + 1)) & 7; }
  DI int order() const {
    const int p = lane_id(), i = p < 3 ? p : p < 10 ? 0 : p < 17 ? 1 : 2, j = prod(p < 3 ? 0 : p - 2 - 7 * i);
    return p < 24 ? (kh * 4 + (j >> 1)) * 7 + (j & 1) * 4 + i : ((p + np) & 7) * 7 + 3;
  }
};
DI unsigned w_voff(int lane) { return (((lane >> 5) * 16 + ((lane >> 2) & 3)) * D_ + ((lane >> 4) & 1) * 32 + (lane & 3) * 8) * 2; }
struct W2Load {
  rsrc_t rw; unsigned vw, lb, so;
  DI W2Load(lds_ptr lds, int w, const char* ws, bool on) {
    if (!on) return;
    const Ffn2 c;
    rw = mkrsrc(ws + WS_W2, F_ * D_ * 2); vw = lane_id() * 16; lb = (unsigned)(size_t)lds + 65536 + w * 4096;
    so = ((c.kh * 4 + (c.np >> 1)) * 7 + (c.np & 1) * 4) * 131072 + c.np * 16384 + (w ^ c.kh * 2) * 4096;
    asm volatile("" : "+v"(vw));
    asm volatile("" : "+s"(rw), "+s"(lb), "+s"(so));
  }
  DI void issue(int t) const { dma64(rw, vw, so + t * 131072, lb + t * 16384); }
};
DI int cs_col(int lane) { return (lane & 7) * 8 + ((lane >> 4) & 1) * 4 + (lane >> 5) * 2 + ((lane >> 3) & 1); }
DI unsigned cs_sel(int lane, int i) { return (lane & 6) == 2 * i ? 0x3f80u << (lane & 1) * 16 : 0u; }
DI void colsum1(const u16* wmat, char* ws, unsigned tag) {
  const Big<1> c;
  const int lane = lane_id();
  const rsrc_t rw = mkrsrc(wmat, 1024u * F_ * 2);
  const unsigned vo = ((lane >> 3) * F_ + (lane & 7) * 8) * 2, so = ((c.mt >> 2) * 256 * F_ + c.col(c.mt & 3)) * 2;
  bf16x8 v[32];
#pragma unroll
  for (int i = 0; i < 32; ++i) v[i] = __builtin_bit_cast(bf16x8, __builtin_amdgcn_raw_buffer_load_b128(rw, vo, so + i * 8 * F_ * 2, 0));
  SBAR();
  u32x4 sel;
#pragma unroll
  for (int i = 0; i < 4; ++i) sel[i] = cs_sel(lane, i);
  f32x4 d = {};
#pragma unroll
  for (int i = 0; i < 32; ++i) d = MFMA16(__builtin_bit_cast(bf16x8, sel), v[i], d);
  const f32x2 e = lane < 32 ? lo2(d) : hi2(d);
  const float r = (lane & 8 ? e.y : e.x) + __builtin_amdgcn_update_dpp(0.f, lane & 8 ? e.x : e.y, 0x128  , 0xf, 0xf, false);
  __builtin_amdgcn_raw_buffer_store_b64(u32x2{__float_as_uint(r), tag}, mkrsrc(ws + WS_CS1, 512 * 1024), lane * 8, (c.nt * 4 + (c.mt & 3)) * 8192 + (c.mt >> 2) * 512, SC1);
}
DI f32x2 ln_coef(float s1, float s2, bool twice) {
  const float mean = s1 * (1.0f / D_), var = __builtin_fmaxf(s2 * (1.0f / D_) - mean * mean, 0.f);
  float r = __builtin_amdgcn_rsqf(var + EPS);
  if (twice) r *= GA * __builtin_amdgcn_rsqf(var * r * r + EPS);
  return f32x2{r, -mean * r};
}
template <int CTRL> DI float dpp_add(float v) { return v + __builtin_amdgcn_update_dpp(0.f, v, CTRL, 0xf, 0xf, false); }

DI void warm(lds_ptr lds, int w, char* ws) {
  const Big<0> c;
  const Big<1> f;
  const Ffn2 e;
  const int lane = lane_id(), b = block_id(), xcd = b & 7, idx = b >> 3, om = OMT, on = b >> 4, k = lane & 7, ly = lane >> 5;
  unsigned o;
  if (w == 0) {
    const int q = lane >> 2 & 3, own = q == 0 ? c.nt * 16 + c.mt : q == 1 ? 256 + c.mt * 16 + c.nt : q == 2 ? 512 + om * 16 + on : 768 + e.mt * 16 + e.nt;
    o = lane & 16 ? ly * WS_L + WS_HF + (f.mt * 16 + f.nt) * 2048 + (lane & 8 ? 8 : k) * 128 : WS_FL + (ly * 1024 + own) * 512 + (lane & 3) * 128;
  } else
    o = lane & 16 ? WS_L + WS_STA + ((e.mt * 4 + (lane >> 2 & 3)) * 16 + e.nt) * 512 + (lane & 3) * 128 : ly * WS_L + WS_STB + ((om * 4 + (lane >> 2 & 3)) * 8 + (on >> 1)) * 512 + (lane & 3) * 128;
  if (WARMP == 3) *(LAS unsigned*)(lds + L_BI + w * 256 + lane * 4) = o;
  else touch(mkrsrc(ws, 2 * WS_L), o, (unsigned)(size_t)lds + L_DUMMY);
}
DI void entry(lds_ptr lds, int wid, char* ws, const u16* wq) {
  if (TQ) {
    const Big<0> c;
    const int w = opq(wid) & 3;
    if ((TQ == 2 || (((unsigned)(size_t)wq >> 7) + c.nt & 7) == 3) && w < 3)
#pragma unroll
      for (int i = 0; i < 2; ++i)
        touch(mkrsrc(wq, 1024 * 3072 * 2), ((((c.mt & 7) * 2 + i) * 64 + lane_id()) * 3072 + w * 1024 + c.nt * 64) * 2, (unsigned)(size_t)lds + L_DUMMY);
    if (TKV && w == 3) {
      const int lane = lane_id();
      touch(mkrsrc(ws + WS_KV, 8 * MB), lane & TKV ? 1u << 31 : ((c.nt * 2 + (lane >> 5)) * 16 + c.mt) * 16384 + (lane & 31) * 512, (unsigned)(size_t)lds + L_DUMMY);
    }
  }
  const int w = opq(wid) & 3, lane = lane_id();
  u64 pc; unsigned n;
  asm volatile("s_getpc_b64 %0\n.Lpc%=:\n\ts_sub_u32 %1, .Lpc%=-.Lkbeg, 0\n\ts_nop 0" : "=s"(pc), "=s"(n) :: "scc");
  pc -= n;
  asm volatile("s_mov_b32 %0, .Lkend-.Lkbeg" : "=s"(n));
  if (CSLP) __builtin_amdgcn_s_sleep(CSLP);
  touch(mkrsrc((const void*)pc, n), (((block_id() >> 3 & 3) * 4 + w) * 64 + lane) * 128, (unsigned)(size_t)lds + L_DUMMY);
  if (WARMP == 0 && (wid & 2) == 0) warm(lds, wid & 1, ws);
}

typedef unsigned u32x16 __attribute__((ext_vector_type(16)));
typedef unsigned u32x32 __attribute__((ext_vector_type(32)));
template <int MODE, bool RAW>
DI void big_load(lds_ptr lds, int w, const void* xf, const u16* wmat, char* ws, unsigned st, bool first, const u16* wo  , char* fl) {
  using C = Big<MODE>;
  w = opq(w);
  const C c;
  const unsigned lds0 = (unsigned)(size_t)lds;
  int lane = lane_id();
  u32x16 ad, sp;
  u32x32 gr;
  const unsigned vw = (((lane >> 5) * (RAW ? 4 : 16) + ((lane >> 2) & 3)) * C::LDW + ((lane >> 4) & 1) * 32 + (lane & 3) * 8) * 2;
#pragma unroll
  for (int i = 0; i < 6; ++i) ad[4 + i] = vw + (c.seg[i >> 1] + (i & 1) * 32 * C::LDW) * 2;
  const int m2 = T0P == 0 ? c.mt : T0P == 1 ? (c.mt & 4) * 2 + (c.mt & 8) / 2 + (c.mt & 3) : T0P == 2 ? (c.mt & 3) * 4 + (c.mt >> 2) : (c.mt & 1) * 8 + (c.mt >> 1);
  const int t0 = RAW ? ((m2 + (c.nt & 12)) & 12) : !MODE && (ROTA || (((unsigned)(size_t)wmat >> 7) + c.nt & 7) == 3) ? c.mt * ROT0 & 15 : 0;
  const rsrc_t rx = mkrsrc((const char*)xf + c.mt * 262144, 262144), rw = mkrsrc(wmat, 1024u * C::LDW * 2);
  unsigned sx = w * (RAW ? 65536 : 4096) + t0 * (RAW ? 128 : 16384), sw = (w * (RAW ? 8 : 4) + t0 * 64) * C::LDW * 2, tmp;
  const unsigned mb = lds0 + C::XB + w * PIECE, mx = lds0 + w * 4096;
  if (RAW) {
#pragma unroll
    for (int f = 0; f < 4; ++f) ad[f] = ((f >> 1) * 16 + (lane >> 2)) * 2048 + (f & 1) * 64 + ((lane ^ -(lane >> 4)) & 3) * 16;
    __builtin_amdgcn_s_waitcnt(0x0F70);
    asm volatile(LOAD_R0 : "+&s"(sx), "+&s"(sw) : "{v[48:63]}"(ad), [rx] "s"(rx), [rw] "s"(rw), [mb] "s"(mb), [mx] "s"(mx) : "memory", "scc", "vcc");
    lane = lane_id();
    ad[10] = WS_STA + (c.mt * 4 + w) * 2048 + (lane >> 5) * 1024 + (lane & 31) * 16;
    sp[1] = WS_FL + (c.nt * 16 + c.mt) * 512 + (w & 1) * 128; sp[2] = WS_EP; sp[13] = lds0 + L_TAG;
    sp[3] = w & 1 ? lds0 + C::XB + (m2 & 3) * 8 * PIECE + (lane & 32) * 16 + (lane & 4) * 64 + (lane & 24) * 8 + (lane & 3) * 16
                  : lds0 + (c.nt & 3) * 4096 + w * 1024 + (lane & 15) * 64 + ((lane >> 4 ^ lane >> 2) & 3) * 16;
    sp[5] = w & 1 ? (w == 1 ? WS_CSQ : 2 * WS_L) + (c.nt * 4 + (m2 & 3)) * 8192 + (m2 >> 2) * 512 + lane * 8
                  : WS_STA + ((c.mt * 4 + (c.nt & 3)) * 4 + (c.nt >> 2)) * 512 + w * 128 + (lane & 15) * 16 + (lane >> 5) * 8;
    sp[14] = 0xffff0000u;
#pragma unroll
    for (int i = 0; i < 4; ++i) gr[i] = cs_sel(lane, i);
  } else {
    ad[0] = lane * 16; ad[1] = st + ((c.mt * 4 + w) * 4096 + (lane >> 5) * 2048) * (MODE ? 1 : 2) + (lane & 31) * 16;
    ad[10] = (((lane >> 4) * 16 + ((lane >> 2) & 3)) * C::LDW + (lane & 3) * 8 + c.ecol) * 2;
  }
  const rsrc_t rs = mkrsrc(ws, 2 * WS_L);
  ad[15] = (MODE ? WS_CS1 : WS_CSQ) + (c.nt * 4 + w) * 8192 + lane * 8;
  unsigned ta = lds0 + C::TRS + (w * 32 + (lane & 31)) * 4, ca = lds0 + C::TCS + (w * 64 + cs_col(lane)) * 4;
  asm volatile("" : "+v"(ta));
  if (RAW) asm volatile("" : "+v"(ca));
  auto granules = [&](unsigned tag, unsigned tagh) {
    unsigned bad = !RAW;
#pragma unroll
    for (int p = 0; p < (RAW ? 8 : 0); ++p) bad |= gr[2 * p + 1] ^ tag;
    if (__builtin_expect(__any(bad != 0), !RAW)) {
      int s = 0;
#pragma nounroll
      do {
        bad = 0;
#pragma unroll
        for (int p = 0; p < (RAW ? 8 : 4); ++p) {
          const u32x2 v = __builtin_amdgcn_raw_buffer_load_b64(rs, opqv(p < 4 ? ad[15] : ad[10]), p < 4 ? p * 512 : (p & 2) * 256 + (p & 1) * 8, SC1);
          gr[2 * p] = v.x; bad |= v.y ^ tag;
        }
        if (!RAW) {
          const u32x2 v = __builtin_amdgcn_raw_buffer_load_b64(rs, opqv((lane_id() & 31) * 512), WS_FL + (256 + Ffn2().np * 32) * 512, SC1);
          bad |= (v.x ^ tag) | (v.y ^ tagh);
        }
      } while (__any(bad != 0) && ++s < SPINS);
    }
  };
  auto colsums = [&] { *(LAS float*)(size_t)ca = (__uint_as_float(gr[0]) + __uint_as_float(gr[2])) + (__uint_as_float(gr[4]) + __uint_as_float(gr[6])); };
  const W2Load w2(lds, w, ws, MODE);
  {
#define LOAD_OP1 : "+&s"(sx), "+&s"(sw), "=&s"(tmp), "+{v[0:31]}"(gr), "+{v[32:47]}"(sp), "+{v[48:63]}"(ad)
#define LOAD_OP2 : [rx] "s"(rx), [rw] "s"(rw), [rs] "s"(rs), [mb] "s"(mb), [mx] "s"(mx)
#define RCL
#define LOAD_OPS LOAD_OP1 LOAD_OP2 : "memory", "scc", "vcc" RCL
    if (RAW) {
      u64 ra;
      asm volatile(LOAD_R LOAD_OP1, [ra] "=&s"(ra) LOAD_OP2, [w] "s"(w) : "memory", "scc", "vcc", LOAD_CLOB RCL);
    } else {
      const Flags<2 - MODE> fg(lds, fl, c.mt, MODE ? w : w >> 1);
      asm volatile("" : "+s"(sx));
      { unsigned sx = 0, sw0 = sw; sw += 3 * 64 * C::LDW * 2; sw = min(sw, sw - 1024 * C::LDW * 2);
        asm volatile("" : "+s"(sw));
        { unsigned sw = sw0; if (MODE) asm volatile(LOAD_F0 LOAD_OPS); else asm volatile(LOAD_Q0 LOAD_OPS); } }
      if (MODE && first) { granules((unsigned)fg.tag, fg.tag >> 32); colsums(); }
      fg.await();
      if (MODE) asm volatile(LOAD_F LOAD_OPS); else asm volatile(LOAD_Q LOAD_OPS);
    }
  }
  STK0;
  constexpr bool KS = KSEL == (RAW ? 0 : MODE ? 1 : 2);
  SBAR();
  if (KS) STK(0);
  SBAR();
  if (LPRIO) __builtin_amdgcn_s_setprio(LPRIO);
  if (MODE) w2.issue(0);
  if (RAW) granules(sp[9], 0);
  f32x2 s;
  auto G = [&](int i, int k) { return f32x2{__uint_as_float(gr[i]), __uint_as_float(gr[i + k])}; };
  auto P = [&](int p) { return MODE ? G(2 * p, 1) : G(4 * p, 1) + G(4 * p + 2, 1); };
  if (RAW) { s = G(8, 2) + G(12, 2); colsums(); }
  else s = ((P(0) + P(1)) + (P(2) + P(3))) + ((P(4) + P(5)) + (P(6) + P(7)));
  auto coef = [&] {
    auto r = __builtin_amdgcn_permlane32_swap(__float_as_uint(s.x), __float_as_uint(s.y), false, false);
    const unsigned t = __float_as_uint(__uint_as_float(r[0]) + __uint_as_float(r[1]));
    r = __builtin_amdgcn_permlane32_swap(t, t, false, false);
    const f32x2 cf = ln_coef(__uint_as_float(r[0]), __uint_as_float(r[1]), MODE == 1);
    *(LAS float*)(size_t)ta = cf.x; *(LAS float*)(size_t)(ta + 512) = cf.y;
  };
  if (!CSPL) coef();
  SBAR();
  if (KS) STK(1);
  wait_vm<MODE ? 4 : 0>(); bar();
  if (KS) STK(2);
  SBAR();
  if (MODE) w2.issue(1);
  if (CSPL) coef();
  SBAR();
  if (KS) STK(3);
  if (LPRIO) __builtin_amdgcn_s_setprio(0);
  bar_lgkm();
  if (KS) STKE;
  if (RAW && w == 2) touch(mkrsrc(wo, D_ * D_ * 2), (block_id() * 64 + lane) * 128, lds0 + L_DUMMY);
  if (WARMP >= 2 && RAW && w < 2) warm(lds, w, ws);
  if (!MODE) bar_lgkm();
}

template <int MODE, class FN>
DI void big_comp(lds_ptr lds, int wid, int l, char* out0, char* xb_  , u32x16& res, FN&& stored) {
  using C = Big<MODE>;
  const unsigned lds0 = (unsigned)(size_t)lds;
  wid = opq(wid);
  const int wm = (wid >> 1) & 1, wn = wid & 1;
  const C c;
  const int mt = c.mt, nt = c.nt, lane = lane_id(), g = lane >> 4, tk = lane & 15;
  const int ta = 4 * wm + 2 * wn;
  f32x32 A0, A1, A2, T;
  f32x16 A3, RM;
  rsrc_t ro = mkrsrc(out0, MODE ? WS_HF - WS_H + MB : S_ * D_ * 4);
  u32x4 ep = {lds0 + C::TRS + (ta * 16 + tk) * 4, lds0 + C::TRS + ((ta ^ 2) * 16 + tk) * 4, lds0 + (MODE ? L_BI : C::TCS) + (wn * 32 + g * 4) * 4,
              MODE ? lane * 16 : lds0 + C::TRS + (ta * 16 + g * 4) * 4};
  unsigned vo[6];
  u32x2 tag;
  if (MODE) {
    vo[0] = lds0 + ta * 2048 + wn * 1024 + lane * 16; vo[1] = lds0 + (ta ^ 2) * 2048 + wn * 1024 + lane * 16;
    vo[2] = (-lane & 1u << 31) | (unsigned)(WS_HF - WS_H + (mt * 16 + nt) * 2048 + (wid & 3) * 128);
    vo[3] = fblk(mt * 8 + ta, c.ecol >> 5, F_) + lane * 16;
    vo[4] = fblk(mt * 8 + ta, c.seg[0] / 32 + wn, F_) + lane * 16; vo[5] = vo[4] ^ 4096;
    tag = *(const LAS u32x2*)(lds + L_TAG);
    asm volatile("" : "+v"(tag));
  } else {
    const unsigned so = ((nt * 2 + wn) * 64 + mt * 4 + (ta >> 1)) * 4096, vq = (g >> 1) * 1024 + (tk + 32 * (g & 1)) * 16;
    vo[0] = so + (g & 1) * 1024 + (tk + 32 * (g >> 1)) * 16; vo[1] = vo[0] ^ 4096;
    vo[2] = lds0 + C::TCS + (128 + wn * 32 + tk) * 4;
    vo[3] = lds0 + L_Q + wn * 8192 + wm * 4096 + vq;
    vo[4] = so + vq; vo[5] = vo[4] ^ 4096;
  }
  asm volatile("" : "+s"(ro));
  asm volatile("" : "+v"(vo[0]), "+v"(vo[1]), "+v"(vo[2]), "+v"(vo[3]), "+v"(vo[4]), "+v"(vo[5]));
  {
    const int sl = MODE == 0 && l == 0 ? (lane & 15) * 4 + ((g ^ -(lane >> 2)) & 3) : lane;
    unsigned xa = lds0 + (4 * wm + 2 * wn) * 2048 + sl * 16, xb = lds0 + (4 * wm + 2 - 2 * wn) * 2048 + sl * 16;
    unsigned ea = lds0 + C::XB + g * PIECE + ((lane >> 2) & 3) * 64 + (lane & 3) * 8, wa = ea + wn * 256;
    ea += C::WB;
    unsigned pat = 0x10410410u, dl, sp = C::SLOT, sn = -2 * C::SLOT;
#define GEMM_ST
#define GEMM_STO
#define GEMM_OPS : "+v"(xa), "+v"(xb), "+v"(wa), "+v"(ea), "+s"(pat), "=&s"(dl), "+s"(sp), "+s"(sn), \
      "=&{v[0:31]}"(A0), "=&{v[32:63]}"(A1), "=&{v[64:95]}"(A2), "=&{v[96:111]}"(A3) GEMM_STO, "=&{v[112:143]}"(T), "=&{v[144:159]}"(RM), "+{v[224:227]}"(ep)
    if (MODE == 0) asm volatile(GEMM_HEAD_Q GEMM_ST GEMM_LOOP_Q GEMM_OPS : : "memory", "scc", GEMM_CLOB);
    else {
      const Ffn2 f;
      const rsrc_t rr = mkrsrc(xb_ + (f.mt * 16 + f.nt) * 16384 + wm * 8192 + wn * 1024, 8192);
      asm volatile(GEMM_HEAD_F GEMM_ST GEMM_LOOP_F GEMM_OPS, "=&{v[208:223]}"(res) : [rr] "s"(rr), [r4] "s"(4096) : "memory", "scc", GEMM_CLOB);
    }
  }
  f32x4 acc[6][4], acce[2][2];
#pragma unroll
  for (int i = 0; i < 6; ++i)
#pragma unroll
    for (int a = 0; a < 4; ++a) {
      const f32x32& v = i < 2 ? A0 : i < 4 ? A1 : A2;
      const int e = ((i & 1) * 4 + a) * 4;
      acc[i][a] = f32x4{v[e], v[e + 1], v[e + 2], v[e + 3]};
    }
#pragma unroll
  for (int i = 0; i < 4; ++i) acce[i >> 1][i & 1] = f32x4{A3[4 * i], A3[4 * i + 1], A3[4 * i + 2], A3[4 * i + 3]};
  ST(MODE ? 13 : 2);
  auto tb = [&](int k) { return f32x4{T[4 * k], T[4 * k + 1], T[4 * k + 2], T[4 * k + 3]}; };
  auto rd4 = [&](unsigned a) { return *(const LAS f32x4*)(size_t)a; };
  float rs[4], mr[4];
#pragma unroll
  for (int a = 0; a < 4; ++a) { rs[a] = RM[2 * a]; mr[a] = RM[8 + 2 * a]; }
  if (MODE == 0) {
    const unsigned e1 = ep[3] + 128 - wn * 256, kv0 = lds0 + L_KV + wn * 16384;
    const f32x4 r1[4] = {rd4(e1), rd4(e1 + 64), rd4(e1 + 512), rd4(e1 + 576)};
    const float cv[2] = {*(const LAS float*)(size_t)vo[2], *(const LAS float*)(size_t)(vo[2] + 64)};
    auto qk = [&](int i) {
      const f32x4 ca = tb(2 * i), cb = tb(2 * i + 1);
#pragma unroll
      for (int a = 0; a < 4; ++a) {
        const f32x2 r2 = bc2(i == 0 ? rs[a] * QSCALE : rs[a]), m2 = bc2(i == 0 ? mr[a] * QSCALE : mr[a]);
        const u32x4 o = {cvtpk2(fma2(lo2(acc[2 * i][a]), r2, lo2(ca) * m2)), cvtpk2(fma2(hi2(acc[2 * i][a]), r2, hi2(ca) * m2)),
                         cvtpk2(fma2(lo2(acc[2 * i + 1][a]), r2, lo2(cb) * m2)), cvtpk2(fma2(hi2(acc[2 * i + 1][a]), r2, hi2(cb) * m2))};
        if (i == 0) *(LAS u32x4*)(size_t)(vo[3] + (wn ^ a >> 1) * 2048 + (a & 1) * 256) = o;
        else { st16(o, ro, vo[4 + (a >> 1)], (a & 1) * 256); *(LAS u32x4*)(size_t)((vo[4 + (a >> 1)] & 16383) + kv0 + (a & 1) * 256) = o; }
      }
    };
    qk(1);
#pragma unroll
    for (int ap = 0; ap < 2; ++ap) {
      const f32x4 ra = ap ? r1[0] : tb(4), rb = ap ? r1[1] : tb(5), ma = ap ? r1[2] : tb(6), mb = ap ? r1[3] : tb(7);
#pragma unroll
      for (int h = 0; h < 2; ++h) {
        const f32x2 c2 = bc2(cv[h]);
        const f32x4 &x0 = acc[4 + h][2 * ap], &x1 = acc[4 + h][2 * ap + 1];
        const u32x4 o4 = {cvtpk2(fma2(lo2(x0), lo2(ra), lo2(ma) * c2)), cvtpk2(fma2(hi2(x0), hi2(ra), hi2(ma) * c2)),
                          cvtpk2(fma2(lo2(x1), lo2(rb), lo2(mb) * c2)), cvtpk2(fma2(hi2(x1), hi2(rb), hi2(mb) * c2))};
        const u32x4 o8 = nat2frag(o4);
        st16(o8, ro, vo[ap], 2048 + h * 256);
        *(LAS u32x4*)(size_t)((vo[ap] & 16383) + kv0 + 2048 + h * 256) = o8;
      }
    }
    SBAR();
    qk(0);
    bar_lgkm();
  } else {
    auto gelu2 = [&](f32x2 x, f32x2 r2, f32x2 o2) {
      const f32x2 s = fma2(x, r2, o2);
      const f32x2 q = s * fma2(s, s, bc2(GB));
      const f32x2 d = fma2(f32x2{__builtin_amdgcn_exp2f(-q.x), __builtin_amdgcn_exp2f(-q.y)}, bc2(GA), bc2(GA));
      return cvtpk2(s * f32x2{__builtin_amdgcn_rcpf(d.x), __builtin_amdgcn_rcpf(d.y)});
    };
    f32x4 t2[2][4];
#pragma unroll
    for (int k = 0; k < 8; ++k) t2[k >> 2][k & 3] = rd4((k >> 2 ? ep[2] - wn * 128 + 768 : ep[2] + 512) + (k & 1) * 64 + (k & 2 ? 0 : 2048));
#pragma unroll
    for (int i = 0; i < 4; ++i) {
      const f32x4 ca = i < 2 ? tb(4 * (i & 1)) : t2[i & 1][0], cb = i < 2 ? tb(4 * (i & 1) + 1) : t2[i & 1][1];
      const f32x4 ba = i < 2 ? tb(4 * (i & 1) + 2) : t2[i & 1][2], bb = i < 2 ? tb(4 * (i & 1) + 3) : t2[i & 1][3];
#pragma unroll
      for (int a = 0; a < (i < 3 ? 4 : 2); ++a) {
        const f32x4 &x0 = i < 3 ? acc[2 * (i % 3)][a] : acce[0][a & 1], &x1 = i < 3 ? acc[2 * (i % 3) + 1][a] : acce[1][a & 1];
        const f32x2 r2 = bc2(rs[a]), m2 = bc2(mr[a]);
        const u32x4 o = {gelu2(lo2(x0), r2, fma2(lo2(ca), m2, lo2(ba))), gelu2(hi2(x0), r2, fma2(hi2(ca), m2, hi2(ba))),
                         gelu2(lo2(x1), r2, fma2(lo2(cb), m2, lo2(bb))), gelu2(hi2(x1), r2, fma2(hi2(cb), m2, hi2(bb)))};
        st16(o, ro, i < 3 ? vo[4 + (a >> 1)] + i * 16384 : vo[3], (a & 1) * 2048);
        if (i < 3) *(LAS u32x4*)(size_t)(vo[a >> 1] + i * 16384 + (a & 1) * 2048) = o;
        if (i * 4 + a + 1 == HF_N)
          asm volatile("s_waitcnt vmcnt(%3)\n\tbuffer_store_dwordx2 %0, %1, %2, 0 offen sc1" ::"v"(tag), "v"(vo[2]), "s"(ro), "i"(HF_N - 4) : "memory");
        stored(i * 4 + a + 1);
      }
    }
  }
}

#define OHM (0x111 << (nt & 12))
#define OWW 2
struct Oproj {
  int mt, nt;
  DI Oproj() { const int b = block_id(), xcd = b & 7, idx = b >> 3; mt = OMT; nt = idx >> 1; }
  DI int tord(int t) const { return t ^ ((~mt & 12) | (nt & 3)); }
  DI u64 poll(char* fl) const { return __builtin_bit_cast(u64, __builtin_amdgcn_raw_buffer_load_b64(mkrsrc(fl + (16 + mt) * 8192, 8192), tord(lane_id()) * 512, 0, SC1)); }
  static DI unsigned seen(u64 f, u64 tag) { unsigned m = (unsigned)__builtin_amdgcn_ballot_w64(f == tag); m &= m >> 1; return m & m >> 2 & 0x1111; }
};
constexpr int L_OM = L_TAG + 8;
DI unsigned oproj_watch(lds_ptr lds, char* fl, int n) {
  const Oproj c;
  const u64 tag = *(const LAS u64*)(lds + L_TAG);
#pragma nounroll
  for (int s = 1; ; ++s) {
    const unsigned m = s < SPINS ? Oproj::seen(c.poll(fl), tag) : 0x1111;
    *(volatile LAS unsigned*)(lds + L_OM) = m;
    if (m == 0x1111 || s == n) return m;
    __builtin_amdgcn_s_sleep(PSLP);
  }
}
DI void oproj_load(lds_ptr lds, int w, const u16* wmat) {
  const Oproj c;
  w = opq(w);
  const rsrc_t rw = mkrsrc(wmat, D_ * D_ * 2);
  const unsigned vw = w_voff(lane_id());
#pragma unroll
  for (int t = 12; t < 16; ++t) {
    const unsigned lb = (unsigned)(size_t)lds + (t * 8 + w) * PIECE, so = (unsigned)(w * 4 * D_ + c.nt * 64 + c.tord(t) * 64 * D_) * 2;
    dma16<0>(rw, vw, so, lb); dma16<0>(rw, vw, so + 32 * D_ * 2, lb + 4 * PIECE);
  }
}
DI void small_store(const f32x4 (&acc)[4][2], int tt, int nt, char* out, char* st, bool last, const Pub& pub) {
  const int lane = lane_id(), tk = lane & 15, g = lane >> 4;
  const rsrc_t ro = mkrsrc(out, S_ * D_ * 2), rs = mkrsrc(st, 16 * S_ * 8);
  f32x2 ts[2];
#pragma unroll
  for (int a = 0; a < 2; ++a) {
    f32x2 s1 = {0.f, 0.f}, s2 = {0.f, 0.f};
    u32x4 o[2];
#pragma unroll
    for (int hh = 0; hh < 2; ++hh)
      o[hh] = u32x4{cvtpk2(lo2(acc[2 * hh][a])), cvtpk2(hi2(acc[2 * hh][a])), cvtpk2(lo2(acc[2 * hh + 1][a])), cvtpk2(hi2(acc[2 * hh + 1][a]))};
    if (last) {
#pragma unroll
      for (int hh = 0; hh < 2; ++hh)
        __builtin_amdgcn_raw_buffer_store_b128(nat2frag(o[hh]), ro, (((tt + a) * 16 + tk) * D_ + nt * 64 + (g & 1) * 16 + (g >> 1) * 8 + hh * 32) * 2, 0, 0);
    } else {
#pragma unroll
      for (int hh = 0; hh < 2; ++hh) {
        st16(o[hh], ro, lane * 16 + a * 2048 + hh * 1024, fblk(tt, nt * 2, D_));
#pragma unroll
        for (int i = 0; i < 4; ++i) { const f32x2 v = bf2(o[hh][i]); s1 += v; s2 = fma2(v, v, s2); }
      }
      ts[a] = f32x2{s1.x + s1.y, s2.x + s2.y};
    }
  }
  if (!last) {
    u32x2 t;
#pragma unroll
    for (int i = 0; i < 2; ++i) {
      const auto r = __builtin_amdgcn_permlane16_swap(__float_as_uint(ts[0][i]), __float_as_uint(ts[1][i]), false, false);
      t[i] = __float_as_uint(swap_add(__uint_as_float(r[0]) + __uint_as_float(r[1])));
    }
    if (lane < 32) __builtin_amdgcn_raw_buffer_store_b64(t, rs, lane * 16, ((tt >> 1) * 8 + (nt >> 1)) * 512 + (nt & 1) * 8, SC1);
  }
  bar();
  pub.go<0>();
}

DI void oproj_comp(lds_ptr lds, int wid, int l, const char* af, const char* resid, char* out, char* st, bool nat, char* fl, unsigned m) {
  const Oproj c;
  wid = opq(wid);
  const int lane = lane_id(), g = lane >> 4, nt = c.nt, tt = c.mt * 8 + (wid & 3) * 2;
  rsrc_t rx = mkrsrc(af + (size_t)c.mt * 256 * D_, 128 * D_ * 2);
  unsigned va = (wid & 3) * 4096 + lane * 16, so, rem = 0x111, t, c0, c1, c2, x = c.tord(0) << 14, hm = OHM;
  unsigned w0 = (unsigned)(size_t)lds + g * PIECE + ((lane >> 2) & 3) * 64 + (lane & 3) * 8, w1;
  ST(7); ST(8);
#define O_OPA
#define O_OPS
  u32x32 fw, fa, fb;
  asm volatile(OPROJ_PRE : "=&s"(so), "+v"(va), "+v"(w0), [rem] "+&s"(rem), [t] "=&s"(t), [c0] "=&s"(c0), "=&{v[96:127]}"(fw), "=&{v[144:175]}"(fa),
               "=&{v[176:207]}"(fb) O_OPA : [m] "s"(m), [rx] "s"(rx), [x] "s"(x), [hm] "s"(hm) : "memory", "scc");
  SBAR();
  const int ln = lane_id(), tk = ln & 15, gr = ln >> 4;
  rsrc_t rr = mkrsrc(resid, S_ * D_ * 2);
  unsigned vr = nat ? tk * 2048 + gr * 16 : ln * 16, r0 = nat ? tt * 32768 + nt * 128 : fblk(tt, nt * 2, D_);
  unsigned r1 = r0 + (nat ? 64 : 1024), r2 = r0 + (nat ? 32768 : 2048), r3 = r1 + (nat ? 32768 : 2048);
  u32x8 sel;
  const int key = nat ? (gr & 1) == tk >> 3 ? (gr >> 1) * 4 + (tk >> 1 & 3) : -9 : gr == tk >> 2 ? (tk >> 1 & 1) : -9;
#pragma unroll
  for (int i = 0; i < 8; ++i) sel[i] = key == (nat ? i : i < 2 ? i : i - 6) ? 0x3f80u << (ln & 1) * 16 : 0u;
  f32x32 A;
  asm volatile(OPROJ_LOOP : "=&s"(so), "+v"(va), "+v"(w0), "=&v"(w1), "+v"(vr), "=&{v[0:31]}"(A), [m] "+&s"(m), [rem] "+&s"(rem), [t] "=&s"(t),
               [c0] "+&s"(c0), [c1] "=&s"(c1), [c2] "=&s"(c2), "+{v[96:127]}"(fw), "+{v[144:175]}"(fa), "+{v[176:207]}"(fb) O_OPS
               : [rx] "s"(rx), [rr] "s"(rr), [r0] "s"(r0), [r1] "s"(r1), [r2] "s"(r2), [r3] "s"(r3), [x] "s"(x), [hm] "s"(hm),
                 [om] "s"((unsigned)(size_t)lds + L_OM), OPROJ_SEL(sel) : "memory", "scc", OPROJ_CLOB);
  const Pub pub(lds, fl, 512 + c.mt * 16 + nt, wid & 3, 0);
  f32x4 acc[4][2];
#pragma unroll
  for (int i = 0; i < 8; ++i) acc[i >> 1][i & 1] = f32x4{A[4 * i], A[4 * i + 1], A[4 * i + 2], A[4 * i + 3]};
  ST(9);
  small_store(acc, tt, nt, out, st, false, pub);
  ST(10);
}

constexpr int L_A4 = 131072;
static_assert(L_DUMMY == 158464, "");
DI void ffn2_load(lds_ptr lds, int w, int l, const char* wmat, const char* h, char* fl) {
  const Ffn2 c;
  w = opq(w);
  const int lane = lane_id(), ord = c.order();
  const u32x2 t = *(const LAS u32x2*)(lds + L_TAG);
  u32x16 ad;
  unsigned la = opq((unsigned)(size_t)lds + w * 4096), lw, s6, s8, s9, s10, s11;
  ad[0] = lane * 16;
  ad[5] = ord * 16384 + w * 4096;
  ad[6] = c.np * 16384 + (lane < 24 ? ord * 131072 + (w ^ c.kh * 2) * 4096 : (((lane + (w >> 1) + c.np) & 7) * 7 + 3) * 131072 + (c.kh * 2 + (w & 1)) * 4096);
  ad[7] = (c.kh * 8 + c.prod(lane) + (lane & 48)) * 2048 + ((w & 2) + (lane >> 3 & 1)) * 128; ad[8] = lane * 2048 + 1024;
  ad[9] = (c.kh * 8 + c.np + (lane | w) * 16) * 2048 + 1024;
  ad[12] = t.x; ad[13] = t.y;
  rsrc_t ra = mkrsrc(h + c.mt * 256 * F_, 256 * F_), rw = mkrsrc(wmat, F_ * D_ * 2), rf = mkrsrc(fl + c.mt * 32768, 32768);
  u64 tag = (u64)(unsigned)__builtin_amdgcn_readfirstlane(t.y) << 32 | (unsigned)__builtin_amdgcn_readfirstlane(t.x), s7;
  asm volatile(FFN2_LOAD : "+s"(ra), "+s"(rw), "+s"(rf), "+s"(la), "=&s"(lw), "+s"(tag), "=&s"(s6), "=&s"(s7), "=&s"(s8), "=&s"(s9), "=&s"(s10), "=&s"(s11),
               "+{v[0:15]}"(ad) : : "memory", "scc", "vcc", FFN2_LCLOB);
}
struct Ffn2Pre { f32x4 b[2]; u32x8 ad; unsigned ex; };
DI void ffn2_pre(Ffn2Pre& p, lds_ptr lds, int wid, int l) {
  wid = opq(wid);
  const int w = wid & 3, wm = w >> 1, wn = w & 1;
  const Ffn2 c;
  const int lane = lane_id(), g = lane >> 4;
  p.b[0] = *(const LAS f32x4*)(lds + L_B2 + wn * 128 + g * 16); p.b[1] = *(const LAS f32x4*)(lds + L_B2 + wn * 128 + 64 + g * 16);
  const u32x2 t = *(const LAS u32x2*)(lds + L_TAG);
  p.ad[0] = (unsigned)(size_t)lds + wm * 8192 + lane * 16;
  p.ad[1] = (unsigned)(size_t)lds + 65536 + wn * 2048 + lane * 16;
  p.ad[2] = w * 8192 + lane * 16;
  p.ad[3] = wm * 8192 + wn * 1024 + lane * 16;
  p.ad[4] = (-lane & 1u << 31) | (unsigned)(l * WS_L + WS_HF + (c.mt * 16 + c.kh * 8 + c.np) * 2048 + w * 128 + 512);
  p.ad[5] = p.ad[0] + L_A4;
  p.ad[6] = t.x; p.ad[7] = t.y;
  p.ex = l * WS_L + WS_P + (c.mt * 16 + c.nt) * 32768;
  asm volatile("" : "+v"(p.ad));
  asm volatile("" : "+s"(p.ex));
}
DI void ffn2_comp(lds_ptr lds, int wid, int l, char* ws0, char* out, char* st, const u32x16& res, Ffn2Pre& p, char* fl) {
  wid = opq(wid);
  const int w = wid & 3, wm = w >> 1, wn = w & 1;
  ST(14); ST(15);
  f32x32 A;
#pragma unroll
  for (int e = 0; e < 8; ++e) A[(e >> 2) * 16 + (e & 3)] = p.b[e >> 2][e & 3];
  u32x32 O;
  u32x8& ad = p.ad;
  rsrc_t rs = mkrsrc(ws0, 2 * WS_L);
  unsigned& ex = p.ex;
  {
    unsigned s2, s3;
#define F2_OPS
    asm volatile(FFN2_LOOP : "+s"(rs), "+s"(ex), "=&s"(s2), "=&s"(s3), "+{v[0:31]}"(A), "=&{v[32:63]}"(O), "+{v[144:151]}"(ad) F2_OPS : : "memory", "scc", FFN2_CLOB);
  }
#pragma unroll
  for (int i = 0; i < 16; ++i) {
    const int e = ((i >> 1 & 1) * 4 + (i >> 2)) * 4 + (i & 1) * 2;
    const f32x2 v = f32x2{A[e], A[e + 1]} + bf2(res[i]);
    A[e] = v.x; A[e + 1] = v.y;
  }
  asm volatile("s_waitcnt vmcnt(0)" : "+{v[0:31]}"(A), "+{v[32:63]}"(O) :: "memory");
  {
    unsigned bad = 0;
#pragma unroll
    for (int q = 0; q < 6; ++q) bad |= O[4 * q + 3] ^ ad[6];
    if (__builtin_expect(__any(bad != 0), 0)) {
      int s = 0;
#pragma nounroll
      do {
        bad = 0;
#pragma unroll
        for (int q = 0; q < 6; ++q) {
          const u32x4 v = __builtin_amdgcn_raw_buffer_load_b128(rs, opqv(ad[2]) + q * 1024, ex, SC1);
          O[4 * q] = v[0]; O[4 * q + 1] = v[1]; O[4 * q + 2] = v[2]; bad |= v[3] ^ ad[6];
        }
      } while (__any(bad != 0) && ++s < SPINS);
    }
  }
  ST(16);
  const Ffn2 c;
  const int lane = lane_id(), tk = lane & 15, g = lane >> 4;
  const rsrc_t ro = mkrsrc(out, S_ * D_ * 2);
  f32x2 ts[4];
#pragma unroll
  for (int a = 0; a < 4; ++a) {
    u32x4 o;
#pragma unroll
    for (int j = 0; j < 4; ++j) {
      const int e = ((j >> 1) * 4 + a) * 4 + (j & 1) * 2;
      o[j] = cvtpk2(f32x2{A[e], A[e + 1]} + bf2(O[e / 6 * 4 + e / 2 % 3]));
    }
    if (l) __builtin_amdgcn_raw_buffer_store_b128(nat2frag(o), ro, (((c.mt * 8 + wm * 4 + a) * 16 + tk) * D_ + c.nt * 64 + wn * 32 + (g & 1) * 16 + (g >> 1) * 8) * 2, 0, OUTP);
    else {
      st16(o, ro, ad[3] + a * 2048, (c.mt * 16 + c.nt) * 16384);
      f32x2 s1 = {0.f, 0.f}, s2 = {0.f, 0.f};
#pragma unroll
      for (int j = 0; j < 4; ++j) { const f32x2 v = bf2(o[j]); s1 += v; s2 = fma2(v, v, s2); }
      ts[a] = f32x2{s1.x + s1.y, s2.x + s2.y};
    }
  }
  if (!l) {
    u32x2 t;
#pragma unroll
    for (int i = 0; i < 2; ++i) {
      const auto r = __builtin_amdgcn_permlane16_swap(__float_as_uint(ts[0][i]), __float_as_uint(ts[1][i]), false, false);
      const auto q = __builtin_amdgcn_permlane16_swap(__float_as_uint(ts[2][i]), __float_as_uint(ts[3][i]), false, false);
      const auto u = __builtin_amdgcn_permlane32_swap(__float_as_uint(__uint_as_float(r[0]) + __uint_as_float(r[1])), __float_as_uint(__uint_as_float(q[0]) + __uint_as_float(q[1])), false, false);
      t[i] = __float_as_uint(__uint_as_float(u[0]) + __uint_as_float(u[1]));
    }
    __builtin_amdgcn_raw_buffer_store_b64(t, mkrsrc(st, 32 * S_ * 8), (lane >> 5) * 8192 + (lane & 31) * 16, ((c.mt * 4 + wm * 2) * 16 + c.nt) * 512 + wn * 8, SC1);
    const Pub pub(lds, fl, 768 + c.mt * 16 + c.nt, wm, wn);
    bar();
    pub.go<0>();
  }
  ST(17);
}

#define PSLEEP 4
#define TCH1 1
#define TCH3N 4
#define TCH3 6
#define TCH5 5
#define RL0 5
#define WOS 2
#define RLD 1
#define USCH 1
#define PRIO_T 11
#define WOP 2
#define PRIO_W 1
#define BAR2 2
#define JOBS 0
#define RLA 1
#define RLB 6
#define TCHD 0
#define OWN 1
#define OWS 3
#define TCH6 2
#define OWF 10
#define K1IN 1
DI unsigned attention(lds_ptr lds, int wid, int l, char* fl, const char* kvf, char* ao, const u16* wo, const u16* w1, const u16* w2, const char* res,
                       const u16* b1, const u16* b2) {
  wid = opq(wid);
#define AD(i)
  AD(20);
  const int lane = lane_id(), kh = wid >> 2, kq = (wid & 2) + kh;
  const unsigned vl = lane * 16;
  const u64 tag = *(const LAS u64*)(lds + L_TAG);
  bf16x8 q[4][2], kb[2][2], vb[2][2];
  const lds_cptr ko = lds + L_KV + (wid & 1) * 16384 + vl;
  kb[1][0] = lds_frag(ko); kb[1][1] = lds_frag(ko + 1024);
#pragma unroll
  for (int i = 0; i < 8; ++i) q[i >> 1][i & 1] = lds_frag(lds + L_Q + (wid & 1) * 8192 + ((vl + (wid >> 1) * 2048) ^ (i >> 1 ^ 3) * 2048) + (i & 1) * 1024);
  kb[0][0] = lds_frag(ko + kq * 4096); kb[0][1] = lds_frag(ko + kq * 4096 + 1024);
  vb[0][0] = lds_frag(ko + kq * 4096 + 2048); vb[0][1] = lds_frag(ko + kq * 4096 + 3072);
  SBAR();
  const Big<0> c;
  const int head = c.nt * 2 + (wid & 1);
  const rsrc_t rf = mkrsrc(fl + c.nt * 8192 + (wid & 1) * 128 + (wid & 2) * 4, 15 * 512 + 8);
  const unsigned tab = opqv((unsigned)(lane ^ c.mt) * 512);
  if (kh)
    asm volatile("s_waitcnt vmcnt(0)\n\tbuffer_store_dwordx2 %0, %1, %2, 0 offen sc1" ::"v"(tag), "v"((vl << 12) + tab), "s"(rf) : "memory");
  AD(21);
  auto pv = [&](int k) { return (((opqv(vl) - k * 16) & ~31u) << 12) + tab; };
  auto poll = [&](int k) { return __builtin_bit_cast(u64, __builtin_amdgcn_raw_buffer_load_b64(rf, pv(k), 0, SC1)); };
  u64 fv = poll(1);
  SBAR();
  rsrc_t rkv = mkrsrc(kvf + (size_t)head * 64 * 4096 + kq * 4096, 61 * 4096);
  auto ldf = [&](unsigned soff, int part) { return __builtin_bit_cast(bf16x8, __builtin_amdgcn_raw_buffer_load_b128(rkv, vl + part * 1024, soff, SC1)); };
  unsigned miss;
  auto seen = [&](u64 f) { miss = (unsigned)__builtin_amdgcn_ballot_w64(f != tag); };
  auto need = [&](int k) {
    if (__builtin_expect((miss >> k & 1) && k < 16, 0)) {
#pragma nounroll
      for (int s = 0; s < SPINS; ++s) { seen(poll(k)); if (!(miss >> k & 1)) break; __builtin_amdgcn_s_sleep(PSLEEP); }
    }
    return (unsigned)(c.mt ^ k) << 14;
  };
  vb[1][1] = bf16x8{};
  asm volatile("" : "+v"(vb[1][1]));
  f32x16 o[4];
  f32x4 ls[4];
#pragma unroll
  for (int a = 0; a < 4; ++a) {
    asm volatile("v_mfma_f32_32x32x16_bf16 %0, %1, %1, 0" : "=v"(o[a]) : "v"(vb[1][1]));
    asm volatile("v_mfma_f32_16x16x32_bf16 %0, %1, %1, 0" : "=v"(ls[a]) : "v"(vb[1][1]));
  }
  if (BAR2 == 0) bar_lgkm();
  AD(22);
  if (TCHO == 2 && !l && wid == 6) touch(mkrsrc(wo, D_ * D_ * 2), (block_id() * 64 + lane) * 128, (unsigned)(size_t)lds + L_DUMMY);
  STL(48); STL0(49);
  AD(23);
  const Oproj p;
  const rsrc_t rw = mkrsrc(wo, D_ * D_ * 2);
  unsigned vw = (((lane >> 5) * 16 + ((lane >> 2) & 3) + (wid & 3) * 4 + kh * 32) * D_ + p.nt * 64 + ((lane >> 4) & 1) * 32 + (lane & 3) * 8) * 2;
  SBAR();
  if (kh == PRIO_W) __builtin_amdgcn_s_setprio(1);
  f32x16 negm;
  {
    f32x16 s[4];
#pragma unroll
    for (int a = 0; a < 4; ++a) s[a] = MFMA32(kb[1][0], q[a][0], f32x16{});
#pragma unroll
    for (int a = 0; a < 4; ++a) s[a] = MFMA32(kb[1][1], q[a][1], s[a]);
    SBAR();
    float m[4];
#pragma unroll
    for (int a = 0; a < 4; ++a) {
      m[a] = __builtin_fmaxf(__builtin_fmaxf(s[0][a], s[0][a + 4]), s[0][a + 8]);
#pragma unroll
      for (int e = 3; e < 15; e += 2) m[a] = __builtin_fmaxf(__builtin_fmaxf(m[a], s[e >> 2][(e & 3) * 4 + a]), s[e + 1 >> 2][(e + 1 & 3) * 4 + a]);
    }
    m[0] = __builtin_fmaxf(__builtin_fmaxf(m[0], m[1]), s[3][12]); m[2] = __builtin_fmaxf(__builtin_fmaxf(m[2], m[3]), s[3][13]);
    m[0] = __builtin_fmaxf(__builtin_fmaxf(m[0], m[2]), __builtin_fmaxf(s[3][14], s[3][15]));
    const f32x2 n = bc2(-swap_max(m[0]));
#pragma unroll
    for (int e = 0; e < 16; e += 2) { f32x2 t; asm("v_mov_b64 %0, %1 ; %2" : "=v"(t) : "v"(n), "i"(e)); negm[e] = t.x; negm[e + 1] = t.y; }
  }
  bf16x8 sel;
  { const unsigned r0 = lane & 4 ? 0u : 0x3f803f80u, r1 = r0 ^ 0x3f803f80u;
    sel = __builtin_bit_cast(bf16x8, u32x4{r0, r0, r1, r1}); }
  int sidx = 0xdddd;
  f32x8 p1 = f32x8{};
  f32x16 sc[2];
  sc[0] = MFMA32(kb[0][0], q[0][0], negm);
  sc[0] = MFMA32(kb[0][1], q[0][1], sc[0]);
  SBAR();
  asm volatile("" : "+v"(negm), "+v"(sidx), "+v"(sel), "+v"(vw));
  AD(24);
  if (BAR2 == 1) bar_lgkm();
  AD(25); AD(26); AD(1);
  unsigned nx = 0;
  auto first = [&] { if (BAR2 == 2) bar_lgkm(); seen(fv); nx = need(1); fv = poll(2); kb[1][0] = ldf(nx, 0); kb[1][1] = ldf(nx, 1); };
  if (!K1IN) first();
  SBAR();
  ST(5); STL0(50);
#define EXPS(lo, hi) do { _Pragma("unroll") for (int e = lo; e < hi; ++e) { \
    float x = __builtin_amdgcn_exp2f(s[e]); asm volatile("" : "+v"(x)); (e < 8 ? p0 : p1)[e & 7] = x; } SBAR(); } while (0)
  auto unit = [&](int a, bf16x8 v0, bf16x8 vp, const bf16x8* k, auto&& mid) {
    const int n = (a + 1) & 3, v = (a + 3) & 3;
    const f32x16& s = sc[a & 1];
    f32x8 p0;
    sc[n & 1] = MFMA32(k[0], q[n][0], negm); SBAR();
    EXPS(0, 3);
    sc[n & 1] = MFMA32(k[1], q[n][1], sc[n & 1]); SBAR();
    EXPS(3, 6);
    o[v] = MFMAF32(vp, p1, o[v]); SBAR();
    EXPS(6, 8);
    ls[v] = MFMAF(sel, p1, ls[v]); SBAR();
    mid(); SBAR();
    EXPS(8, 10);
    o[a] = MFMAF32(v0, p0, o[a]); SBAR();
    EXPS(10, 13);
    ls[a] = MFMAF(sel, p0, ls[a]); SBAR();
    EXPS(13, 16);
  };
#define tw (JOBS ? kh ? 9 : TCH1 + TCHD * (wid >> 1) : TCH1 + (wid >> 1))
#define rl (JOBS ? kh ? RLB : RLA : RL0)
  const unsigned jobs = opq((l ? 0u : 1u << tw | 1u << rl | 1u << rl + RLD | 1u << TCH5 | (TCHO == 3)) | 1u << TCH3);
  for (int it = 0; it < 8; ++it) {
    int ito = it;
    asm volatile("" : "+s"(ito));
    if (__builtin_expect(jobs >> ito & 1, 0)) {
      if (!l && ito == tw)
        touch(mkrsrc(JOBS || (wid & 4) ? w1 : w2, F_ * D_ * 2), ((block_id() * 4 + (wid & 3)) * 64 + lane_id()) * 128, (unsigned)(size_t)lds + L_DUMMY);
      if (!l && wid < 7 && (ito == rl || ito == rl + RLD)) {
        const Big<0> d;
        const int ln = lane_id(), f = d.mt * 16 + d.nt, wv = opq(wid), u = (f & 31) * 7 + wv;
        const unsigned lb = (unsigned)(size_t)lds + L_XCH + wv * 6144;
        if (ito == rl) {
          const rsrc_t r = mkrsrc(w2, F_ * D_ * 2);
          const unsigned v = w_voff(ln), so = (((u >> 2) * 64 + (u & 1) * 32) * D_ + (f >> 5) * 128 + (u & 2) * 32) * 2;
#pragma unroll
          for (int g = 0; g < 4; ++g) dma16<0>(r, v, so + g * 4 * D_ * 2, lb + g * PIECE);
        } else {
          wait_vm<8>();
          const rsrc_t r = mkrsrc(opq(kvf) - WS_KV + WS_W2 + ((u >> 2) * 32 + (f >> 5) * 4 + (u & 3)) * 4096, 4096);
          const lds_cptr a = (lds_cptr)(size_t)(lb + (ln >> 4) * PIECE + ((ln >> 2) & 3) * 64 + (ln & 3) * 8);
#pragma unroll
          for (int i = 0; i < 2; ++i) {
            const bf16x8 u0 = cat8(lds_tr(a + i * 256), lds_tr(a + i * 256 + 512)), u1 = cat8(lds_tr(a + i * 256 + 32), lds_tr(a + i * 256 + 544));
            st16(__builtin_bit_cast(u32x4, u0), r, ln * 16, i * 2048);
            st16(__builtin_bit_cast(u32x4, u1), r, ln * 16, i * 2048 + 1024);
          }
        }
      }
      if (TCHO == 3 && !l && !ito && wid == 6) touch(mkrsrc(wo, D_ * D_ * 2), (block_id() * 64 + lane_id()) * 128, (unsigned)(size_t)lds + L_DUMMY);
      if (WARMP == 3 && !l && ito == TCH5 && wid < 2)
        touch(mkrsrc(kvf - WS_KV, 2 * WS_L), *(const LAS unsigned*)(lds + L_BI + wid * 256 + lane_id() * 4), (unsigned)(size_t)lds + L_DUMMY);
      if (ito == TCH3) {
        const int b = block_id(), ln = lane_id();
        touch(mkrsrc(w1, TCH3N * 64 * F_ * 2), ((b >> 3) + wid * 32 + (ln >> 5) * 256) * (F_ * 2) + (b & 4) * 896 + ((ln & 31) < 27 ? (ln & 31) : 27) * 128, (unsigned)(size_t)lds + L_DUMMY);
      }
    }
    SBAR();
#pragma unroll
    for (int c = 0; c < 2; ++c) {
      auto wot = [&] {
        const unsigned t = (ito - WOS) * 2 + c;
        asm volatile("s_cmp_lt_u32 %4, 12\n\ts_cbranch_scc0 .Lo%=\n\ts_mov_b32 m0, %0\n\ts_nop 0\n\tbuffer_load_dwordx4 %1, %2, %3 offen lds\n.Lo%=:"
                     ::"s"((unsigned)(size_t)lds + (t * 8 + wid) * PIECE), "v"(vw), "s"(rw), "s"(p.tord(t & 15) * (64 * D_ * 2)), "s"(t) : "memory", "scc"); };
      AD(ito * 2 + c + 2);
      if (c == (PRIO_T & 1) && __builtin_expect(ito == PRIO_T / 2, 0)) __builtin_amdgcn_s_setprio(0);
      unit(0, vb[c][0], vb[c ^ 1][1], kb[c], [&] { if (K1IN && !c && __builtin_expect(!ito, 0)) first(); vb[c ^ 1][0] = ldf(nx, 2); vb[c ^ 1][1] = ldf(nx, 3); });
      unit(1, vb[c][0], vb[c][1], kb[c], [&] { if (WOP == 1) wot(); });
      unit(2, vb[c][0], vb[c][1], kb[c], [&] {
        if (!c) seen(fv);
        nx = need(ito * 2 + 2 + c);
        if (WOP == 2) { wot(); SBAR(); }
        kb[c][0] = ldf(nx, 0); kb[c][1] = ldf(nx, 1);
        if (c) fv = poll(ito * 2 + 4);
      });
      unit(3, vb[c][0], vb[c][1], kb[c ^ 1], [&] { if (WOP == 3) wot(); });
    }
  }
#undef tw
#undef rl
  o[3] = MFMAF32(vb[1][1], p1, o[3]);
  ls[3] = MFMAF(sel, p1, ls[3]);
  ST(6); STL0(51);
  if (TCH6 && kh == TCH6 - 1) {
    const Oproj d;
    const Big<1> e;
    const int ln = lane_id(), w = wid & 3, r = w * 32 + (ln & 31);
    const unsigned du = (unsigned)(size_t)lds + L_DUMMY;
    touch(mkrsrc(wo, D_ * D_ * 2), (d.tord(12 + w) * 64 + ln) * (D_ * 2) + d.nt * 128, du);
    touch(mkrsrc(res, S_ * D_ * 2), l ? (d.mt * 16 + d.nt) * 16384 + r * 128 : (d.mt * 128 + r) * 2048 + d.nt * 128, du);
    if (!l) {
      touch(mkrsrc(w1, D_ * F_ * 2), ((e.mt >> 2) * 256 + w * 64 + ln) * (F_ * 2) + e.col(e.mt & 3) * 2, du);
      touch(mkrsrc(wid & 1 ? b1 : b2, wid & 1 ? F_ * 2 : D_ * 2), ln * 128, du);
    }
  }
  SBAR();
#pragma unroll
  for (int a = 0; a < 3; ++a) {
    const lds_ptr d = lds + L_XCH + (wid * 3 + a) * 2048 + vl;
    *(LAS u32x4*)d = pack8(o[a], 0); *(LAS u32x4*)(d + 1024) = pack8(o[a], 1);
  }
  const lds_ptr xl = lds + L_XCH + 49152 + (vl & 511);
  *(LAS f32x4*)(xl + wid * 512) = f32x4{ls[0][0], ls[1][0], ls[2][0], 0.f};
  if (!TCH6 || kh != TCH6 - 1) wait_vm<0>(); else if (l) wait_vm<2>(); else wait_vm<4>();
  bar_lgkm();
  float xs[3];
  u32x4 xv[6];
#pragma unroll
  for (int a = 0; a < 3; ++a) {
    xs[a] = *(const LAS float*)(xl + (wid ^ (a ^ 3) * 2) * 512 + a * 4);
#pragma unroll
    for (int h = 0; h < 2; ++h) xv[2 * a + h] = *(const LAS u32x4*)(lds + L_XCH + ((wid ^ (a ^ 3) * 2) * 3 + a) * 2048 + h * 1024 + vl);
  }
  SBAR();
  const u64 fo = Oproj().poll(fl);
  const rsrc_t ro = mkrsrc(ao, S_ * D_ * 2);
  const unsigned vo = opqv(((lane >> 4) & 1) * 2048 + ((lane & 15) + 16 * (lane >> 5)) * 16 + fblk((c.mt * 4 + (wid >> 1)) * 2, head, D_));
  SBAR();
  f32x16 of = o[3];
  float lf = ls[3][0] + xs[0] + xs[1] + xs[2];
#pragma unroll
  for (int a = 0; a < 6; ++a) {
#pragma unroll
    for (int i = 0; i < 4; ++i) { of[8 * (a & 1) + 2 * i] += bflo(xv[a][i]); of[8 * (a & 1) + 2 * i + 1] += bfhi(xv[a][i]); }
    if (a & 1) SBAR();
  }
  f32x2 chk[2] = {};
#pragma unroll
  for (int e = 0; e < 8; ++e) asm("v_pk_fma_f32 %0, %1, 0, %0" : "+v"(chk[e & 1]) : "v"(f32x2{of[2 * e], of[2 * e + 1]}));
  chk[0] += chk[1];
  const bool bad = !(lf < 1.0e18f) || !(lf + chk[0].x + chk[0].y >= 1.0e-18f);
  if (__builtin_expect(__any(bad), 0)) {
    rkv = mkrsrc(kvf + (size_t)head * 64 * 4096, 64 * 4096);
    float m = -3.0e38f;
    of = f32x16{}; lf = 0.f;
    for (int j = 0; j < 64; ++j) {
      f32x16 s = MFMA32(ldf(j * 4096, 0), q[3][0], f32x16{});
      s = MFMA32(ldf(j * 4096, 1), q[3][1], s);
      float mx = s[0];
#pragma unroll
      for (int e = 1; e < 16; ++e) mx = __builtin_fmaxf(mx, s[e]);
      mx = __builtin_fmaxf(swap_max(mx), m);
      const float f = __builtin_amdgcn_exp2f(m - mx);
      m = mx;
      float ps = 0.f;
#pragma unroll
      for (int e = 0; e < 16; ++e) { s[e] = bfhi(__float_as_uint(__builtin_amdgcn_exp2f(s[e] - m))); ps += s[e]; of[e] *= f; }
      lf = lf * f + swap_add(ps);
      of = MFMAF32(ldf(j * 4096, 2), __builtin_shufflevector(s, s, 0, 1, 2, 3, 4, 5, 6, 7), of);
      of = MFMAF32(ldf(j * 4096, 3), __builtin_shufflevector(s, s, 8, 9, 10, 11, 12, 13, 14, 15), of);
    }
  }
  const float rl = __builtin_amdgcn_rcpf(lf);
#pragma unroll
  for (int e = 0; e < 16; ++e) of[e] *= rl;
  st16(u32x4{cvtpk(of[0], of[1]), cvtpk(of[2], of[3]), cvtpk(of[8], of[9]), cvtpk(of[10], of[11])}, ro, vo, 0);
  st16(u32x4{cvtpk(of[4], of[5]), cvtpk(of[6], of[7]), cvtpk(of[12], of[13]), cvtpk(of[14], of[15])}, ro, vo + 512, 0);
  SBAR();
  return Oproj::seen(fo, tag);
}

__global__ __launch_bounds__(512, 2) void layers_kernel(const u16* __restrict__ x, const u16* __restrict__ wqkv, const u16* __restrict__ wo,
                                                        const u16* __restrict__ w1, const u16* __restrict__ b1, const u16* __restrict__ w2,
                                                        const u16* __restrict__ b2, char* ws0, char* out) {
  __shared__ __attribute__((aligned(16))) char lds_[L_END];
  const lds_ptr lds = (lds_ptr)lds_;
  const int wid = __builtin_amdgcn_readfirstlane(threadIdx.x >> 6);
  const bool loader = wid < 4;
  asm volatile(".Lkbeg:");
  if (!loader) entry(lds, wid, ws0, wqkv);
#pragma nounroll
  for (int l = 0; l < 2; ++l) {
    char* ws = opq(ws0) + l * WS_L;
    char* fl = opq(ws0) + WS_FL + l * 1024 * 512;
    if (loader) {
      if (l) big_load<0, false>(lds, wid, ws + WS_XA, wqkv, opq(ws0), WS_L + WS_STA, false, nullptr, fl - 16 * 8192);
      else big_load<0, true>(lds, wid, x, wqkv, opq(ws0), 0, true, wo, nullptr);
    } else {
      ST(18);
      ST(0);
      { u32x16 u; big_comp<0>(lds, wid, l, ws + WS_KV, fl, u, [](int) {}); }
      ST(3);
    }
    ST(4);
    unsigned om = attention(lds, wid, l, fl, ws + WS_KV, ws + WS_AO, wo, w1, w2, l ? ws + WS_XA : (const char*)x, b1, b2);
    if (loader) {
      const Pub pa(lds, fl, 256 + Big<0>().mt * 16 + Big<0>().nt, 0, 0);
      if (wid == OWW) *(volatile LAS unsigned*)(lds + L_OM) = om;
      __builtin_amdgcn_s_waitcnt(0x0070); bar();
      if (wid == 0) pa.go<0>();
      if (wid == OWW) { if (om != 0x1111) om = oproj_watch(lds, fl, OWN); }
      else if (om & 0x111) __builtin_amdgcn_s_sleep(OWF);
      else __builtin_amdgcn_s_sleep(OWS);
      oproj_load(lds, wid, wo);
      if (wid == OWW && om != 0x1111) oproj_watch(lds, fl, 0);
      if (!l) {
        const int lane = lane_id();
        const unsigned bi = __builtin_amdgcn_raw_buffer_load_b16(mkrsrc(b1, F_ * 2), lane * 2, Big<1>().col(wid) * 2, 0);
        const unsigned bo = __builtin_amdgcn_raw_buffer_load_b16(mkrsrc(b2, D_ * 2), lane * 2, Ffn2().nt * 128, 0);
        if (wid == 3) colsum1(w1, opq(ws0), *(const LAS unsigned*)(lds + L_TAG));
        *(LAS float*)(lds + L_BI + (wid * 64 + lane) * 4) = bflo(bi) * GA;
        *(LAS float*)(lds + L_B2 + lane * 4) = bflo(bo);
      } else wait_vm<0>();
      big_load<1, false>(lds, wid, ws + WS_XB, w1, opq(ws0), l * WS_L + WS_STB, !l, nullptr, fl + 32 * 8192);
      ffn2_load(lds, wid, l, opq(ws0) + WS_W2, ws + WS_H, ws + WS_HF);
    } else {
      oproj_comp(lds, wid, l, ws + WS_AO, l ? ws + WS_XA : (const char*)x, ws + WS_XB, ws + WS_STB, !l, fl, om);
      ST(11);
      u32x16 res;
      Ffn2Pre pre;
      big_comp<1>(lds, wid, l, ws + WS_H, ws + WS_XB, res, [&](int n) { if (n == PRE_N) ffn2_pre(pre, lds, wid, l); });
      ffn2_comp(lds, wid, l, opq(ws0), l ? out : ws + WS_L + WS_XA, ws + WS_L + WS_STA, res, pre, fl);
    }
  }
  if (blockIdx.x == 0 && threadIdx.x == 0) __hip_atomic_store((gu64*)(opq(ws0) + WS_EP), *(const LAS u64*)(lds + L_TAG), __ATOMIC_RELAXED, __HIP_MEMORY_SCOPE_AGENT);
  asm volatile(".Lkend:");
}

}

extern "C" void kernel_launch(void* const* d_in, const int* in_sizes, int n_in, void* d_out, int out_size, void* d_ws, size_t ws_size,
                              hipStream_t stream) {
  layers_kernel<<<256, 512, 0, stream>>>((const u16*)d_in[0], (const u16*)d_in[1], (const u16*)d_in[2], (const u16*)d_in[4], (const u16*)d_in[5],
                                         (const u16*)d_in[6], (const u16*)d_in[7], (char*)d_ws, (char*)d_out);
}
```
